```python
import torch

def _softmax(x, axis=-1):
    return torch.softmax(x, dim=axis)
def _gelu(x):
    return torch.nn.functional.gelu(x, approximate="tanh")
def _silu(x):
    return torch.nn.functional.silu(x)
def _sigmoid(x):
    return torch.sigmoid(x)
def _layernorm(x, axis=-1, eps=1e-5):
    m = x.mean(dim=axis, keepdim=True)
    v = x.var(dim=axis, keepdim=True, unbiased=False)
    return (x - m) / torch.sqrt(v + eps)
def _scatter_add(tgt, idx, src, axis=0):
    return tgt.clone().index_add_(axis, idx, src)
def _segment_sum(src, seg, n):
    out = torch.zeros((n,) + src.shape[1:], dtype=src.dtype)
    return out.index_add_(0, seg, src)
def _pad(x, pw):
    flat = tuple(v for pair in reversed(pw) for v in pair)
    return torch.nn.functional.pad(x, flat)

kernel_name = 'sequential_preLN__mha_block__mixer_token_mlp__prompt'

def setup_inputs():

    _w = (lambda a: (lambda t: t.to(torch.bfloat16) if t.is_floating_point() else t)(torch.as_tensor(a)))
    def _A_setup():
        B, S, D, H = 1, 2048, 640, 32
        return {
            "x": _w((lambda *s: torch.randn(*s, dtype=torch.bfloat16) * 0.05)(B, S, D)),
            "wqkv": _w((lambda *s: torch.randn(*s, dtype=torch.bfloat16) * 0.05)(D, 3 * D)),
            "wo": _w((lambda *s: torch.randn(*s, dtype=torch.bfloat16) * 0.05)(D, D)),
        }
    _A_in = _A_setup()

    def _B_setup():
        B, S, D, F = 1, 2048, 640, 1536
        return {
            "x": _w((lambda *s: torch.randn(*s, dtype=torch.bfloat16) * 0.05)(B, S, D)),
            "w1": _w((lambda *s: torch.randn(*s, dtype=torch.bfloat16) * 1.6)(S, F) * (S ** -0.5)),
            "b1": _w((lambda *s: torch.randn(*s, dtype=torch.bfloat16) * 1.6)(F) * 0.02),
            "w2": _w((lambda *s: torch.randn(*s, dtype=torch.bfloat16) * 0.2)(F, S) * (F ** -0.5)),
            "b2": _w((lambda *s: torch.randn(*s, dtype=torch.bfloat16) * 0.2)(S) * 0.02),
        }
    _B_in = _B_setup()

    return {
        "A_x": _A_in["x"],
        "A_wqkv": _A_in["wqkv"],
        "A_wo": _A_in["wo"],
        "B_x": _B_in["x"],
        "B_w1": _B_in["w1"],
        "B_b1": _B_in["b1"],
        "B_w2": _B_in["w2"],
        "B_b2": _B_in["b2"],
    }

def reference(A_x, A_wqkv, A_wo, B_x, B_w1, B_b1, B_w2, B_b2):
    def _A_ref(x, wqkv, wo):
        B, S, D = x.shape
        H = 32
        Dh = D // H
        qkv = (lambda x, *d: x.permute(*d))((x @ wqkv).reshape(B, S, 3, H, Dh), 2, 0, 3, 1, 4)
        q, k, v = qkv[0], qkv[1], qkv[2]
        scores = torch.einsum("bhqd,bhkd->bhqk", q, k) * (Dh ** -0.5)
        p = _softmax(scores, axis=-1)
        o = torch.einsum("bhqk,bhkd->bhqd", p, v)
        return (lambda x, *d: x.permute(*d))(o, 0, 2, 1, 3).reshape(B, S, D) @ wo

    def _B_ref(x, w1, b1, w2, b2):
        B, S, D = x.shape
        h = _layernorm(x, axis=-1)
        u = torch.einsum("bsd,sf->bdf", h, w1) + b1
        u = _gelu(u)
        y = torch.einsum("bdf,fs->bds", u, w2) + b2
        return (lambda x, *d: x.permute(*d))(y, 0, 2, 1)

    x = A_x
    h = x + _A_ref(_layernorm(x, axis=-1), A_wqkv, A_wo)
    h2 = h + _B_ref(_layernorm(h, axis=-1), B_w1, B_b1, B_w2, B_b2)
    return h2
```

```cpp
#include <hip/hip_runtime.h>
#include <hip/hip_bf16.h>
#include <stdint.h>

#define LAS __attribute__((address_space(3)))
#define DI __device__ __forceinline__
typedef __attribute__((ext_vector_type(8))) short bf16x8;
typedef __attribute__((ext_vector_type(4))) short bf16x4;
typedef __attribute__((ext_vector_type(2))) __bf16 bf16x2v;
typedef __attribute__((ext_vector_type(16))) float f32x16;
typedef __attribute__((ext_vector_type(4))) float f32x4;
typedef __attribute__((ext_vector_type(4))) unsigned u32x4;
typedef __attribute__((ext_vector_type(2))) unsigned u32x2;
typedef __amdgpu_buffer_rsrc_t rsrc_t;

constexpr int SEQ = 2048, DM = 640, NH = 32, FF = 1536;
constexpr float EPS = 1e-5f;
constexpr float QSCALE = 0.22360679774997896f * 1.4426950408889634f;

constexpr size_t WS_KV = 0;
constexpr size_t WS_Q0 = WS_KV + (size_t)NH * 64 * 3072;
constexpr size_t WS_Q1 = WS_Q0 + (size_t)NH * 64 * 1024;
constexpr size_t WS_O  = WS_Q1 + (size_t)NH * SEQ * 8;
constexpr size_t WS_H  = WS_O + (size_t)SEQ * DM * 2;
constexpr size_t WS_HN = WS_H + (size_t)SEQ * DM * 2;
constexpr size_t WS_U  = WS_HN + (size_t)SEQ * DM * 2;
constexpr size_t WS_ST = WS_U + (size_t)DM * FF * 2;
constexpr size_t WS_ST_BYTES = (size_t)32 * 8 * 64 * 16;
constexpr size_t WS_END = WS_ST + WS_ST_BYTES;

DI rsrc_t mk_rsrc(const void* p, unsigned bytes) { return __builtin_amdgcn_make_buffer_rsrc((void*)p, (short)0, (int)bytes, 0x00020000); }
DI void dma16(unsigned lds, unsigned voff, rsrc_t rs, unsigned soff) {
    asm volatile("s_mov_b32 m0, %0\n\ts_nop 0\n\tbuffer_load_dwordx4 %1, %2, %3 offen lds" ::"s"(lds), "v"(voff), "s"(rs), "s"(soff) : "memory");
}
template <int N> DI void wait_bar() { asm volatile("s_waitcnt vmcnt(%0) lgkmcnt(0)\n\ts_barrier" ::"i"(N) : "memory"); }
DI void lds_bar() { asm volatile("s_waitcnt lgkmcnt(0)\n\ts_barrier" ::: "memory"); }
DI unsigned cvtpk(float lo, float hi) {
    typedef __attribute__((ext_vector_type(2))) float f32x2;
    return __builtin_bit_cast(unsigned, __builtin_convertvector(f32x2{lo, hi}, bf16x2v));
}
DI float bf_lo(unsigned u) { return __uint_as_float(u << 16); }
DI float bf_hi(unsigned u) { return __uint_as_float(u & 0xffff0000u); }
DI bf16x4 ldtr(const LAS char* p) { return __builtin_amdgcn_ds_read_tr16_b64_v4i16((LAS bf16x4*)p); }
DI bf16x8 ldtr2(const LAS char* p0, const LAS char* p1) {
    const bf16x4 a = ldtr(p0), b = ldtr(p1);
    return __builtin_shufflevector(a, b, 0, 1, 2, 3, 4, 5, 6, 7);
}
DI bf16x8 ld128(const LAS char* p) { return *(const LAS bf16x8*)p; }
DI f32x16 mfma32(bf16x8 a, bf16x8 b, f32x16 c) { return __builtin_amdgcn_mfma_f32_32x32x16_bf16(a, b, c, 0, 0, 0); }
DI f32x16 mfma32k8(bf16x4 a, bf16x4 b, f32x16 c) { return __builtin_amdgcn_mfma_f32_32x32x8bf16_1k(a, b, c, 0, 0, 0); }
DI f32x4 mfma16(bf16x8 a, bf16x8 b, f32x4 c) { return __builtin_amdgcn_mfma_f32_16x16x32_bf16(a, b, c, 0, 0, 0); }
DI float dot2(unsigned a, unsigned b, float c) {
    return __builtin_amdgcn_fdot2_f32_bf16(__builtin_bit_cast(bf16x2v, a), __builtin_bit_cast(bf16x2v, b), c, false);
}
DI float xhalf_sum(float v) {
    auto r = __builtin_amdgcn_permlane32_swap(__float_as_uint(v), __float_as_uint(v), false, false);
    return __uint_as_float(r[0]) + __uint_as_float(r[1]);
}
DI float xhalf_max(float v) {
    auto r = __builtin_amdgcn_permlane32_swap(__float_as_uint(v), __float_as_uint(v), false, false);
    return __builtin_fmaxf(__uint_as_float(r[0]), __uint_as_float(r[1]));
}
DI int uni(int v) { return __builtin_amdgcn_readfirstlane(v); }

struct Params {
    const uint16_t* x; const uint16_t* wqkv; const uint16_t* wo; const uint16_t* w1; const uint16_t* b1; const uint16_t* w2; const uint16_t* b2;
    uint16_t* out; char* ws;
};

constexpr int A_STAGE = 32768, A_NS = 4, A_NT = 10, A_STAT = A_STAGE * A_NS;
constexpr int A_LDS = A_STAT + 2 * 128 * 8 + 4 * 128 * 4;

template <bool ISV>
DI void phaseA_body(const Params& p, LAS char* sm, int rb, int ct) {
    const int tid = threadIdx.x, l = tid & 63, w = uni(tid >> 6);
    const int wm = w >> 1, wn = w & 1, r = l & 31, h = l >> 5;
    const int row0 = rb * 128, col0 = ct * 128;
    const unsigned sm0 = (unsigned)(uintptr_t)sm;
    const bool isA = w < 4;
    const rsrc_t rs = mk_rsrc(isA ? (const void*)p.x : (const void*)p.wqkv, isA ? SEQ * DM * 2 : DM * 3 * DM * 2);
    unsigned voff[4];
#pragma unroll
    for (int j = 0; j < 4; ++j) {
        const int u = (w & 3) * 4 + j;
        const int ra = 8 * u + (l >> 3), ca = (l & 7) ^ ((ra >> 1) & 7);
        const int kb = 4 * u + (l >> 4), cb = (l & 15) ^ ((kb & 3) << 2);
        voff[j] = isA ? (unsigned)((row0 + ra) * (DM * 2) + ca * 16) : (unsigned)(kb * (3 * DM * 2) + col0 * 2 + cb * 16);
    }
    const unsigned kstep = isA ? 128u : 64u * (3 * DM * 2);
    const unsigned ldst = sm0 + (isA ? 0 : 16384) + (w & 3) * 4096;
    auto issue = [&](int t) {
#pragma unroll
        for (int j = 0; j < 4; ++j) dma16(ldst + (t % A_NS) * A_STAGE + j * 1024, voff[j], rs, kstep * t);
    };
    issue(0); issue(1); issue(2);
    const int f = wm ^ (2 * wn);
    const int g = l >> 4, q = (l & 15) >> 2, p4 = l & 3;
    unsigned xa[4], wa[4];
    const unsigned xbase = (32 * wm + r) * 128 + ((h ^ ((r >> 1) & 7)) << 4);
    const int cw = 8 * wn + 2 * (g & 1) + (p4 >> 1);
#pragma unroll
    for (int ps = 0; ps < 4; ++ps) {
        const int ks = ps ^ f;
        xa[ps] = xbase ^ (ks << 5);
        wa[ps] = 16384 + (16 * ks + 8 * (g >> 1) + q) * 256 + ((cw ^ (q << 2)) << 4) + (p4 & 1) * 8;
    }
    f32x16 acc[2] = {};
    float xs = 0.f, xq = 0.f, cs0 = 0.f, cs1 = 0.f;
    const unsigned ONES = 0x3f803f80u;
#pragma unroll
    for (int t = 0; t < A_NT; ++t) {
        if (t + 2 < A_NT) wait_bar<8>(); else if (t + 1 < A_NT) wait_bar<4>(); else wait_bar<0>();
        if (t + 3 < A_NT) issue(t + 3);
        const LAS char* st = sm + (t % A_NS) * A_STAGE;
#pragma unroll
        for (int ps = 0; ps < 4; ++ps) {
            const bf16x8 xf = ld128(st + xa[ps]);
            const bf16x8 w0 = ldtr2(st + wa[ps], st + wa[ps] + 1024);
            const bf16x8 w1 = ldtr2(st + (wa[ps] ^ 64), st + (wa[ps] ^ 64) + 1024);
            if (ISV) { acc[0] = mfma32(xf, w0, acc[0]); acc[1] = mfma32(xf, w1, acc[1]); }
            else     { acc[0] = mfma32(w0, xf, acc[0]); acc[1] = mfma32(w1, xf, acc[1]); }
            if (ps < 2) {
                const u32x4 xu = __builtin_bit_cast(u32x4, xf);
#pragma unroll
                for (int i = 0; i < 4; ++i) { xs = dot2(xu[i], ONES, xs); xq = dot2(xu[i], xu[i], xq); }
            }
            if (ps == 0) {
                const u32x4 a = __builtin_bit_cast(u32x4, w0), b = __builtin_bit_cast(u32x4, w1);
#pragma unroll
                for (int i = 0; i < 4; ++i) { cs0 = dot2(a[i], ONES, cs0); cs1 = dot2(b[i], ONES, cs1); }
            }
        }
    }
    xs = xhalf_sum(xs); xq = xhalf_sum(xq); cs0 = xhalf_sum(cs0); cs1 = xhalf_sum(cs1);
    LAS float* xst = (LAS float*)(sm + A_STAT);
    LAS float* cst = (LAS float*)(sm + A_STAT + 2048);
    if (h == 0) {
        xst[(wn * 128 + 32 * wm + r) * 2] = xs; xst[(wn * 128 + 32 * wm + r) * 2 + 1] = xq;
        cst[wm * 128 + 64 * wn + r] = cs0; cst[wm * 128 + 64 * wn + 32 + r] = cs1;
    }
    lds_bar();
    const int T = rb * 4 + wm;
    char* ws = p.ws;
    if (ISV) {
        float mean[16], rstd[16];
#pragma unroll
        for (int i = 0; i < 16; ++i) {
            const int tk = 32 * wm + (i & 3) + 8 * (i >> 2) + 4 * h;
            const float s = xst[tk * 2] + xst[(128 + tk) * 2], ss = xst[tk * 2 + 1] + xst[(128 + tk) * 2 + 1];
            const float m = s * (1.f / DM);
            mean[i] = m; rstd[i] = __builtin_amdgcn_rsqf(fmaxf(ss * (1.f / DM) - m * m, 0.f) + EPS);
        }
#pragma unroll
        for (int j = 0; j < 2; ++j) {
            const int cl = 64 * wn + 32 * j + r;
            const float cs = cst[cl] + cst[128 + cl] + cst[256 + cl] + cst[384 + cl];
            const int c = col0 + cl - 2 * DM, head = c / 20, d = c - head * 20;
            char* dst = ws + WS_KV + (size_t)(head * 64 + T) * 3072 + 1536 + h * 384 + d * 16;
#pragma unroll
            for (int s = 0; s < 2; ++s) {
                u32x4 v;
#pragma unroll
                for (int i = 0; i < 4; ++i) {
                    const int a = 8 * s + 2 * i;
                    v[i] = cvtpk(rstd[a] * (acc[j][a] - mean[a] * cs), rstd[a + 1] * (acc[j][a + 1] - mean[a + 1] * cs));
                }
                *(u32x4*)(dst + s * 768) = v;
                if (d == 19) *(u32x4*)(dst + s * 768 + 16) = u32x4{0x3f803f80u, 0x3f803f80u, 0x3f803f80u, 0x3f803f80u};
            }
        }
    } else {
        const int tk = 32 * wm + r;
        const float s = xst[tk * 2] + xst[(128 + tk) * 2], ss = xst[tk * 2 + 1] + xst[(128 + tk) * 2 + 1];
        const float m = s * (1.f / DM);
        const bool isQ = ct < 5;
        const float rstd = __builtin_amdgcn_rsqf(fmaxf(ss * (1.f / DM) - m * m, 0.f) + EPS) * (isQ ? QSCALE : 1.f);
#pragma unroll
        for (int j = 0; j < 2; ++j)
#pragma unroll
            for (int gg = 0; gg < 4; ++gg) {
                const int cl = 64 * wn + 32 * j + 8 * gg + 4 * h;
                f32x4 cs = *(const LAS f32x4*)(cst + cl);
                cs += *(const LAS f32x4*)(cst + 128 + cl); cs += *(const LAS f32x4*)(cst + 256 + cl); cs += *(const LAS f32x4*)(cst + 384 + cl);
                u32x2 v;
                v[0] = cvtpk(rstd * (acc[j][4 * gg] - m * cs[0]), rstd * (acc[j][4 * gg + 1] - m * cs[1]));
                v[1] = cvtpk(rstd * (acc[j][4 * gg + 2] - m * cs[2]), rstd * (acc[j][4 * gg + 3] - m * cs[3]));
                const int c4 = (col0 + cl - (isQ ? 0 : DM)) >> 2, head = c4 / 5, grp = c4 - head * 5;
                if (isQ) {
                    char* dst = grp < 4 ? ws + WS_Q0 + ((size_t)(head * 64 + T) * 64 + (grp >> 1) * 32 + r) * 16 + (grp & 1) * 8
                                        : ws + WS_Q1 + ((size_t)head * SEQ + row0 + tk) * 8;
                    *(u32x2*)dst = v;
                } else {
                    char* base = ws + WS_KV + (size_t)(head * 64 + T) * 3072;
                    char* dst = grp < 4 ? base + ((grp >> 1) * 32 + r) * 16 + (grp & 1) * 8 : base + 1024 + r * 8;
                    *(u32x2*)dst = v;
                    if (grp == 4) *(u32x2*)(base + 1280 + r * 8) = u32x2{0x3f803f80u, 0x00003f80u};
                }
            }
    }
}
DI void phaseA(const Params& p, LAS char* sm, int b) {
    const int g = b & 7, i = b >> 3, rb = 4 * (g & 3) + (i & 3), ct = (g >> 2) * 8 + (i >> 2);
    if (ct >= 15) return;
    if (ct >= 10) phaseA_body<true>(p, sm, rb, ct); else phaseA_body<false>(p, sm, rb, ct);
}

constexpr int B_STAGE = 8 * 3072, B_NS = 3, B_NT = 8, B_FLAG = B_STAGE * B_NS;
constexpr int B_LDS = B_FLAG + 64;

DI void phaseB(const Params& p, LAS char* sm, int b) {
    const int tid = threadIdx.x, l = tid & 63, w = uni(tid >> 6), r = l & 31, h = l >> 5;
    const int g = b & 7, i = b >> 3, head = 4 * g + (i >> 3), T = (i & 7) * 8 + w;
    const unsigned sm0 = (unsigned)(uintptr_t)sm;
    const rsrc_t rs = mk_rsrc(p.ws + WS_KV + (size_t)head * 64 * 3072, 64 * 3072);
    const unsigned voff = w * 3072 + l * 16;
    auto issue = [&](int t, int slot) {
#pragma unroll
        for (int j = 0; j < 3; ++j) dma16(sm0 + slot * B_STAGE + w * 3072 + j * 1024, voff, rs, t * B_STAGE + j * 1024);
    };
    const bf16x8 q0 = *(const bf16x8*)(p.ws + WS_Q0 + ((size_t)(head * 64 + T) * 64 + l) * 16);
    u32x2 q1u = *(const u32x2*)(p.ws + WS_Q1 + ((size_t)head * SEQ + T * 32 + r) * 8);
    if (h) q1u = u32x2{0u, 0u};
    const unsigned k0o = l * 16, k1o = 1024 + l * 8, vo = 1536 + h * 384 + (r < 24 ? r : r - 16) * 16;
    f32x16 o;
    float lsum;
    for (int pass = 0;; ++pass) {
        if (pass == 1) {
            if (h) q1u = u32x2{0u, 0u};
            float mx = -INFINITY;
            issue(0, 0); issue(1, 1);
            for (int t = 0; t < B_NT; ++t) {
                if (t + 1 < B_NT) wait_bar<3>(); else wait_bar<0>();
                if (t + 2 < B_NT) issue(t + 2, (t + 2) % B_NS);
                const LAS char* st = sm + (t % B_NS) * B_STAGE;
                for (int kt = 0; kt < 8; ++kt) {
                    f32x16 s = mfma32(ld128(st + kt * 3072 + k0o), q0, f32x16{});
                    s = mfma32k8(*(const LAS bf16x4*)(st + kt * 3072 + k1o), __builtin_bit_cast(bf16x4, q1u), s);
#pragma unroll
                    for (int e = 0; e < 16; ++e) mx = __builtin_fmaxf(mx, s[e]);
                }
            }
            mx = xhalf_max(mx);
            lds_bar();
            const unsigned hi = cvtpk(mx, 0.f) & 0xffffu; const float r1 = mx - bf_lo(hi);
            const unsigned mid = cvtpk(r1, 0.f) & 0xffffu; const float r2 = r1 - bf_lo(mid);
            const unsigned lo = cvtpk(r2, 0.f) & 0xffffu;
            if (h) q1u = u32x2{(hi | (mid << 16)) ^ 0x80008000u, lo ^ 0x8000u};
        }
        issue(0, 0); issue(1, 1);
        if (pass == 0) {
            wait_bar<3>();
            f32x16 s = mfma32(ld128(sm + k0o), q0, f32x16{});
            s = mfma32k8(*(const LAS bf16x4*)(sm + k1o), __builtin_bit_cast(bf16x4, q1u), s);
            float mx = s[0];
#pragma unroll
            for (int e = 1; e < 16; ++e) mx = __builtin_fmaxf(mx, s[e]);
            mx = xhalf_max(mx);
            const unsigned hi = cvtpk(mx, 0.f) & 0xffffu; const float r1 = mx - bf_lo(hi);
            const unsigned mid = cvtpk(r1, 0.f) & 0xffffu; const float r2 = r1 - bf_lo(mid);
            const unsigned lo = cvtpk(r2, 0.f) & 0xffffu;
            if (h) q1u = u32x2{(hi | (mid << 16)) ^ 0x80008000u, lo ^ 0x8000u};
        }
        const bf16x4 q1 = __builtin_bit_cast(bf16x4, q1u);
        o = f32x16{};
        int slot = 0;
        for (int t = 0; t < B_NT; ++t) {
            if (t + 1 < B_NT) wait_bar<3>(); else wait_bar<0>();
            const int nslot = slot == 0 ? 2 : slot - 1;
            if (t + 2 < B_NT) issue(t + 2, nslot);
            const LAS char* st = sm + slot * B_STAGE;
#pragma unroll
            for (int kt = 0; kt < 8; ++kt) {
                const LAS char* tp = st + kt * 3072;
                f32x16 s = mfma32(ld128(tp + k0o), q0, f32x16{});
                s = mfma32k8(*(const LAS bf16x4*)(tp + k1o), q1, s);
                u32x4 p0, p1;
#pragma unroll
                for (int e = 0; e < 4; ++e) {
                    p0[e] = cvtpk(__builtin_amdgcn_exp2f(s[2 * e]), __builtin_amdgcn_exp2f(s[2 * e + 1]));
                    p1[e] = cvtpk(__builtin_amdgcn_exp2f(s[8 + 2 * e]), __builtin_amdgcn_exp2f(s[8 + 2 * e + 1]));
                }
                o = mfma32(ld128(tp + vo), __builtin_bit_cast(bf16x8, p0), o);
                o = mfma32(ld128(tp + vo + 768), __builtin_bit_cast(bf16x8, p1), o);
            }
            slot = slot == 2 ? 0 : slot + 1;
        }
        {
            auto rr = __builtin_amdgcn_permlane32_swap(__float_as_uint(o[8]), __float_as_uint(o[8]), false, false);
            lsum = __uint_as_float(rr[1]);
        }
        if (pass == 1) break;
        bool ok = lsum > 0x1p-30f && lsum < 0x1p64f;
#pragma unroll
        for (int e = 0; e < 12; ++e) ok = ok && (e >= 8 && h ? true : __builtin_fabsf(o[e]) < INFINITY);
        LAS int* flag = (LAS int*)(sm + B_FLAG);
        const bool wave_ok = __all(ok);
        if (l == 0) flag[w] = wave_ok ? 0 : 1;
        lds_bar();
        const int bad = flag[0] | flag[1] | flag[2] | flag[3] | flag[4] | flag[5] | flag[6] | flag[7];
        if (!uni(bad)) break;
        lds_bar();
    }
    const float inv = 1.f / lsum;
    char* orow = p.ws + WS_O + (size_t)(T * 32 + r) * (DM * 2);
#pragma unroll
    for (int e = 0; e < 3; ++e) {
        if (e < 2 || h == 0) {
            const int gam = head * 5 + 2 * e + h;
            const int G = gam & 7, pos = (gam & ~7) + 2 * (G & 3) + (G >> 2);
            u32x2 v;
            v[0] = cvtpk(o[4 * e] * inv, o[4 * e + 1] * inv); v[1] = cvtpk(o[4 * e + 2] * inv, o[4 * e + 3] * inv);
            *(u32x2*)(orow + pos * 8) = v;
        }
    }
}

constexpr int C_STAGE = 36864, C_NS = 4, C_NT = 5, C_RED = C_STAGE * C_NS;
constexpr int C_LDS = C_RED + 8 * 64 * 8 + 64;

DI void phaseC(const Params& p, LAS char* sm, int b) {
    const int tid = threadIdx.x, l = tid & 63, w = uni(tid >> 6);
    const int gx = b & 7, ix = b >> 3, rb = 4 * gx + (ix >> 3), cb = ix & 7;
    const int row0 = rb * 64, col0 = cb * 80;
    const unsigned sm0 = (unsigned)(uintptr_t)sm;
    const rsrc_t rsO = mk_rsrc(p.ws + WS_O, SEQ * DM * 2), rsW = mk_rsrc(p.wo, DM * DM * 2);
    unsigned vo[2], vw[3];
#pragma unroll
    for (int j = 0; j < 2; ++j) { const int u = w * 2 + j, row = 4 * u + (l >> 4), c = (l & 15) ^ (row & 15); vo[j] = (row0 + row) * (DM * 2) + c * 16; }
#pragma unroll
    for (int j = 0; j < 3; ++j) { const int u = j * 8 + w, ch = u * 64 + l, kr = ch / 10, c = ch - kr * 10; vw[j] = kr * (DM * 2) + col0 * 2 + c * 16; }
    auto issue = [&](int t) {
        const unsigned base = sm0 + (t % C_NS) * C_STAGE;
#pragma unroll
        for (int j = 0; j < 2; ++j) dma16(base + (w * 2 + j) * 1024, vo[j], rsO, t * 256);
#pragma unroll
        for (int j = 0; j < 2; ++j) dma16(base + 16384 + (j * 8 + w) * 1024, vw[j], rsW, t * 128 * (DM * 2));
        if (w < 4) dma16(base + 16384 + (16 + w) * 1024, vw[2], rsW, t * 128 * (DM * 2));
    };
    issue(0); issue(1); issue(2);
    const int mi = w & 3, kh = w >> 2, tk = l & 15, g = l >> 4, q = (l & 15) >> 2, p4 = l & 3;
    const unsigned oa = (16 * mi + tk) * 256, wa = 16384 + (4 * g + q) * 160 + p4 * 8;
    f32x4 acc[5] = {};
#pragma unroll
    for (int t = 0; t < C_NT; ++t) {
        if (w < 4) { if (t + 2 < C_NT) wait_bar<10>(); else if (t + 1 < C_NT) wait_bar<5>(); else wait_bar<0>(); }
        else       { if (t + 2 < C_NT) wait_bar<8>();  else if (t + 1 < C_NT) wait_bar<4>(); else wait_bar<0>(); }
        if (t + 3 < C_NT) issue(t + 3);
        const LAS char* st = sm + (t % C_NS) * C_STAGE;
#pragma unroll
        for (int s2 = 0; s2 < 2; ++s2) {
            const int ks = 2 * kh + s2;
            const bf16x8 of = ld128(st + oa + (((4 * ks + g) ^ tk) << 4));
            const LAS char* wp = st + wa + ks * 32 * 160;
#pragma unroll
            for (int j = 0; j < 5; ++j) acc[j] = mfma16(ldtr2(wp + j * 32, wp + j * 32 + 16 * 160), of, acc[j]);
        }
    }
    lds_bar();
    LAS f32x4* red = (LAS f32x4*)sm;
    if (kh == 1) {
#pragma unroll
        for (int j = 0; j < 5; ++j) red[(mi * 5 + j) * 64 + l] = acc[j];
    }
    lds_bar();
    const int row = row0 + 16 * mi + tk;
    float hv[20];
    LAS float* fin = (LAS float*)(sm + C_RED);
    unsigned long long* gran = (unsigned long long*)(p.ws + WS_ST) + (size_t)rb * 8 * 64 * 2;
    if (kh == 0) {
        float s = 0.f, ss = 0.f;
#pragma unroll
        for (int j = 0; j < 5; ++j) {
            const f32x4 a = acc[j] + red[(mi * 5 + j) * 64 + l];
            const u32x2 xv = *(const u32x2*)(p.x + (size_t)row * DM + col0 + 16 * j + 4 * g);
            u32x2 hb;
            hb[0] = cvtpk(a[0] + bf_lo(xv[0]), a[1] + bf_hi(xv[0])); hb[1] = cvtpk(a[2] + bf_lo(xv[1]), a[3] + bf_hi(xv[1]));
            *(u32x2*)(p.ws + WS_H + ((size_t)row * DM + col0 + 16 * j + 4 * g) * 2) = hb;
            hv[4 * j] = bf_lo(hb[0]); hv[4 * j + 1] = bf_hi(hb[0]); hv[4 * j + 2] = bf_lo(hb[1]); hv[4 * j + 3] = bf_hi(hb[1]);
#pragma unroll
            for (int e = 0; e < 4; ++e) { s += hv[4 * j + e]; ss += hv[4 * j + e] * hv[4 * j + e]; }
        }
        s += __shfl_xor(s, 16); ss += __shfl_xor(ss, 16); s += __shfl_xor(s, 32); ss += __shfl_xor(ss, 32);
        if (g < 2) {
            const unsigned long long v = (1ull << 32) | __float_as_uint(g == 0 ? s : ss);
            __hip_atomic_store(gran + ((size_t)cb * 64 + 16 * mi + tk) * 2 + g, v, __ATOMIC_RELAXED, __HIP_MEMORY_SCOPE_AGENT);
        }
    }
    {
        unsigned long long a, c; unsigned spins = 0;
        for (;;) {
            a = __hip_atomic_load(gran + ((size_t)w * 64 + l) * 2, __ATOMIC_RELAXED, __HIP_MEMORY_SCOPE_AGENT);
            c = __hip_atomic_load(gran + ((size_t)w * 64 + l) * 2 + 1, __ATOMIC_RELAXED, __HIP_MEMORY_SCOPE_AGENT);
            if (__all((a >> 32) == 1ull && (c >> 32) == 1ull)) break;
            if (++spins > (1u << 20)) break;
            __builtin_amdgcn_s_sleep(1);
        }
        fin[(w * 64 + l) * 2] = __uint_as_float((unsigned)a); fin[(w * 64 + l) * 2 + 1] = __uint_as_float((unsigned)c);
    }
    lds_bar();
    if (kh == 0) {
        float s = 0.f, ss = 0.f;
#pragma unroll
        for (int j = 0; j < 8; ++j) { s += fin[(j * 64 + 16 * mi + tk) * 2]; ss += fin[(j * 64 + 16 * mi + tk) * 2 + 1]; }
        const float m = s * (1.f / DM), var = fmaxf(ss * (1.f / DM) - m * m, 0.f);
        const float r1 = 1.f / sqrtf(var + EPS);
        const float r2 = 1.f / sqrtf(var * r1 * r1 + EPS), rr = r1 * r2;
#pragma unroll
        for (int j = 0; j < 5; ++j) {
            u32x2 v;
            v[0] = cvtpk((hv[4 * j] - m) * rr, (hv[4 * j + 1] - m) * rr); v[1] = cvtpk((hv[4 * j + 2] - m) * rr, (hv[4 * j + 3] - m) * rr);
            *(u32x2*)(p.ws + WS_HN + ((size_t)row * DM + col0 + 16 * j + 4 * g) * 2) = v;
        }
    }
}

constexpr int D_STAGE = 32768, D_NS = 4, D_NT = 16;
constexpr int D_LDS = D_STAGE * D_NS;

DI float gelu_tanh(float v) {
    const float y = v * (0.7978845608028654f + 0.7978845608028654f * 0.044715f * v * v);
    return v * __builtin_amdgcn_rcpf(1.f + __builtin_amdgcn_exp2f(-2.f * 1.4426950408889634f * y));
}
DI void phaseD(const Params& p, LAS char* sm, int b) {
    const int gx = b & 7, ix = b >> 3;
    if (ix >= 30) return;
    const int db = 5 * (gx & 1) + ix % 5, fb = 6 * (gx >> 1) + ix / 5;
    const int tid = threadIdx.x, l = tid & 63, w = uni(tid >> 6);
    const unsigned sm0 = (unsigned)(uintptr_t)sm;
    const bool isA = w < 4;
    const int pitch = isA ? DM * 2 : FF * 2;
    const rsrc_t rs = mk_rsrc(isA ? (const void*)(p.ws + WS_HN) : (const void*)p.w1, isA ? SEQ * DM * 2 : SEQ * FF * 2);
    const int kr = 32 * (w & 3) + (l >> 3);
    const unsigned voff = kr * pitch + (isA ? db : fb) * 128 + (((l & 7) ^ (((kr >> 1) & 1) << 2)) << 4);
    const unsigned ldst = sm0 + (isA ? 0 : 16384) + (w & 3) * 4096;
    auto issue = [&](int t, int slot) {
#pragma unroll
        for (int j = 0; j < 4; ++j) dma16(ldst + slot * D_STAGE + j * 1024, voff, rs, (unsigned)((t * 128 + j * 8) * pitch));
    };
    issue(0, 0); issue(1, 1); issue(2, 2);
    const int kh = w >> 2, wf = (w >> 1) & 1, wd = w & 1;
    const int g = l >> 4, q = (l & 15) >> 2, p4 = l & 3;
    const unsigned rowo = (64 * kh + 8 * (g >> 1) + q) * 128 + (p4 & 1) * 8, sw = ((q >> 1) & 1) << 2;
    const unsigned ha = rowo + (((4 * wd + 2 * (g & 1) + (p4 >> 1)) ^ sw) << 4);
    const unsigned wa = 16384 + rowo + (((4 * wf + 2 * (g & 1) + (p4 >> 1)) ^ sw) << 4);
    f32x16 acc = {};
    int slot = 0;
    for (int t = 0; t < D_NT; ++t) {
        if (t + 2 < D_NT) wait_bar<8>(); else if (t + 1 < D_NT) wait_bar<4>(); else wait_bar<0>();
        if (t + 3 < D_NT) issue(t + 3, (slot + 3) & 3);
        const LAS char* st = sm + slot * D_STAGE;
#pragma unroll
        for (int ks = 0; ks < 4; ++ks) {
            const bf16x8 hf = ldtr2(st + ha + ks * 2048, st + ha + ks * 2048 + 512);
            const bf16x8 wf_ = ldtr2(st + wa + ks * 2048, st + wa + ks * 2048 + 512);
            acc = mfma32(wf_, hf, acc);
        }
        slot = (slot + 1) & 3;
    }
    lds_bar();
    LAS f32x4* red = (LAS f32x4*)sm;
    if (kh == 1) {
#pragma unroll
        for (int e = 0; e < 4; ++e) red[((w & 3) * 4 + e) * 64 + l] = f32x4{acc[4 * e], acc[4 * e + 1], acc[4 * e + 2], acc[4 * e + 3]};
    }
    lds_bar();
    if (kh == 0) {
        const int r = l & 31, h = l >> 5, d = db * 64 + 32 * wd + r;
#pragma unroll
        for (int e = 0; e < 4; ++e) {
            const f32x4 o = red[((w & 3) * 4 + e) * 64 + l];
            const int f0 = fb * 64 + 32 * wf + 8 * e + 4 * h;
            const u32x2 bv = *(const u32x2*)(p.b1 + f0);
            u32x2 v;
            v[0] = cvtpk(gelu_tanh(acc[4 * e] + o[0] + bf_lo(bv[0])), gelu_tanh(acc[4 * e + 1] + o[1] + bf_hi(bv[0])));
            v[1] = cvtpk(gelu_tanh(acc[4 * e + 2] + o[2] + bf_lo(bv[1])), gelu_tanh(acc[4 * e + 3] + o[3] + bf_hi(bv[1])));
            *(u32x2*)(p.ws + WS_U + ((size_t)d * FF + f0) * 2) = v;
        }
    }
}

constexpr int E_STAGE = 36864, E_NS = 4, E_NT = 12;
constexpr int E_LDS = E_STAGE * E_NS;

DI void phaseE(const Params& p, LAS char* sm, int b) {
    const int gx = b & 7, ix = b >> 3, db = 4 * (gx & 1) + (ix & 3), sb = 8 * (gx >> 1) + (ix >> 2);
    const int d0 = db * 80, s0 = sb * 64;
    const int tid = threadIdx.x, l = tid & 63, w = uni(tid >> 6);
    const unsigned sm0 = (unsigned)(uintptr_t)sm;
    const rsrc_t rsU = mk_rsrc(p.ws + WS_U, DM * FF * 2), rsW = mk_rsrc(p.w2, FF * SEQ * 2);
    unsigned vu[3], vw[2];
#pragma unroll
    for (int j = 0; j < 3; ++j) { const int u = j * 8 + w, row = 4 * u + (l >> 4), c = (l & 15) ^ (row & 15); vu[j] = (d0 + row) * (FF * 2) + c * 16; }
#pragma unroll
    for (int j = 0; j < 2; ++j) {
        const int u = w * 2 + j, kr = 8 * u + (l >> 3), fr = ((kr >> 1) & 1) | (((kr >> 3) & 1) << 1);
        vw[j] = kr * (SEQ * 2) + s0 * 2 + (((l & 7) ^ (fr << 1)) << 4);
    }
    auto issue = [&](int t, int slot) {
        const unsigned base = sm0 + slot * E_STAGE;
#pragma unroll
        for (int j = 0; j < 2; ++j) dma16(base + (j * 8 + w) * 1024, vu[j], rsU, t * 256);
        if (w < 4) dma16(base + (16 + w) * 1024, vu[2], rsU, t * 256);
#pragma unroll
        for (int j = 0; j < 2; ++j) dma16(base + 20480 + (w * 2 + j) * 1024, vw[j], rsW, (unsigned)(t * 128 * (SEQ * 2)));
    };
    issue(0, 0); issue(1, 1); issue(2, 2);
    const int si = w & 3, kh = w >> 2, tk = l & 15, g = l >> 4, q = (l & 15) >> 2, p4 = l & 3;
    const int fr = ((q >> 1) & 1) | ((g & 1) << 1);
    const unsigned wa = 20480 + (8 * g + q) * 128 + (((2 * si + (p4 >> 1)) ^ (fr << 1)) << 4) + (p4 & 1) * 8;
    f32x4 acc[5] = {};
    int slot = 0;
    for (int t = 0; t < E_NT; ++t) {
        if (w < 4) { if (t + 2 < E_NT) wait_bar<10>(); else if (t + 1 < E_NT) wait_bar<5>(); else wait_bar<0>(); }
        else       { if (t + 2 < E_NT) wait_bar<8>();  else if (t + 1 < E_NT) wait_bar<4>(); else wait_bar<0>(); }
        if (t + 3 < E_NT) issue(t + 3, (slot + 3) & 3);
        const LAS char* st = sm + slot * E_STAGE;
#pragma unroll
        for (int s2 = 0; s2 < 2; ++s2) {
            const int ks = 2 * kh + s2;
            const bf16x8 wf = ldtr2(st + wa + ks * 4096, st + wa + ks * 4096 + 512);
            const unsigned uo = tk * 256 + (((4 * ks + g) ^ tk) << 4);
#pragma unroll
            for (int j = 0; j < 5; ++j) acc[j] = mfma16(ld128(st + uo + j * 4096), wf, acc[j]);
        }
        slot = (slot + 1) & 3;
    }
    lds_bar();
    LAS f32x4* red = (LAS f32x4*)sm;
    if (kh == 1) {
#pragma unroll
        for (int j = 0; j < 5; ++j) red[(si * 5 + j) * 64 + l] = acc[j];
    }
    lds_bar();
    if (kh == 0) {
        const int s = s0 + 16 * si + tk;
        const float bias = bf_lo((unsigned)p.b2[s]);
#pragma unroll
        for (int j = 0; j < 5; ++j) {
            const f32x4 a = acc[j] + red[(si * 5 + j) * 64 + l];
            const size_t off = (size_t)s * DM + d0 + 16 * j + 4 * g;
            const u32x2 hv = *(const u32x2*)(p.ws + WS_H + off * 2);
            u32x2 v;
            v[0] = cvtpk(bf_lo(hv[0]) + (a[0] + bias), bf_hi(hv[0]) + (a[1] + bias));
            v[1] = cvtpk(bf_lo(hv[1]) + (a[2] + bias), bf_hi(hv[1]) + (a[3] + bias));
            *(u32x2*)(p.out + off) = v;
        }
    }
}

__global__ void __launch_bounds__(512, 2) k_phaseA(Params p) {
    extern __shared__ __attribute__((aligned(16))) char smem[];
    { const int i = blockIdx.x * 512 + threadIdx.x; if (i < (int)(WS_ST_BYTES / 16)) *(u32x4*)(p.ws + WS_ST + (size_t)i * 16) = u32x4{0u, 0u, 0u, 0u}; }
    phaseA(p, (LAS char*)smem, blockIdx.x);
}
__global__ void __launch_bounds__(512, 2) k_phaseB(Params p) { extern __shared__ __attribute__((aligned(16))) char smem[]; phaseB(p, (LAS char*)smem, blockIdx.x); }
__global__ void __launch_bounds__(512, 2) k_phaseC(Params p) { extern __shared__ __attribute__((aligned(16))) char smem[]; phaseC(p, (LAS char*)smem, blockIdx.x); }
__global__ void __launch_bounds__(512, 2) k_phaseD(Params p) { extern __shared__ __attribute__((aligned(16))) char smem[]; phaseD(p, (LAS char*)smem, blockIdx.x); }
__global__ void __launch_bounds__(512, 2) k_phaseE(Params p) { extern __shared__ __attribute__((aligned(16))) char smem[]; phaseE(p, (LAS char*)smem, blockIdx.x); }

extern "C" void kernel_launch(void* const* d_in, const int* in_sizes, int n_in, void* d_out, int out_size, void* d_ws, size_t ws_size,
                              hipStream_t stream) {
    Params p;
    p.x = (const uint16_t*)d_in[0]; p.wqkv = (const uint16_t*)d_in[1]; p.wo = (const uint16_t*)d_in[2];
    p.w1 = (const uint16_t*)d_in[4]; p.b1 = (const uint16_t*)d_in[5]; p.w2 = (const uint16_t*)d_in[6]; p.b2 = (const uint16_t*)d_in[7];
    p.out = (uint16_t*)d_out; p.ws = (char*)d_ws;
    (void)hipFuncSetAttribute((const void*)k_phaseA, hipFuncAttributeMaxDynamicSharedMemorySize, A_LDS);
    (void)hipFuncSetAttribute((const void*)k_phaseB, hipFuncAttributeMaxDynamicSharedMemorySize, B_LDS);
    (void)hipFuncSetAttribute((const void*)k_phaseC, hipFuncAttributeMaxDynamicSharedMemorySize, C_LDS);
    (void)hipFuncSetAttribute((const void*)k_phaseD, hipFuncAttributeMaxDynamicSharedMemorySize, D_LDS);
    (void)hipFuncSetAttribute((const void*)k_phaseE, hipFuncAttributeMaxDynamicSharedMemorySize, E_LDS);
    k_phaseA<<<256, 512, A_LDS, stream>>>(p);
    k_phaseB<<<256, 512, B_LDS, stream>>>(p);
    k_phaseC<<<256, 512, C_LDS, stream>>>(p);
    k_phaseD<<<256, 512, D_LDS, stream>>>(p);
    k_phaseE<<<256, 512, E_LDS, stream>>>(p);
}
```

```cpp
#include <hip/hip_runtime.h>
#include <hip/hip_bf16.h>
#include <stdint.h>

#define LAS __attribute__((address_space(3)))
#define DI __device__ __forceinline__
typedef __attribute__((ext_vector_type(8))) short bf16x8;
typedef __attribute__((ext_vector_type(4))) short bf16x4;
typedef __attribute__((ext_vector_type(2))) __bf16 bf16x2v;
typedef __attribute__((ext_vector_type(16))) float f32x16;
typedef __attribute__((ext_vector_type(4))) float f32x4;
typedef __attribute__((ext_vector_type(4))) unsigned u32x4;
typedef __attribute__((ext_vector_type(2))) unsigned u32x2;
typedef __amdgpu_buffer_rsrc_t rsrc_t;

constexpr int SEQ = 2048, DM = 640, NH = 32, FF = 1536;
constexpr float EPS = 1e-5f;
constexpr float QSCALE = 0.22360679774997896f * 1.4426950408889634f;

constexpr size_t WS_KV = 0;
constexpr size_t WS_Q0 = WS_KV + (size_t)NH * 64 * 3072;
constexpr size_t WS_Q1 = WS_Q0 + (size_t)NH * 64 * 1024;
constexpr size_t WS_O  = WS_Q1 + (size_t)NH * SEQ * 8;
constexpr size_t WS_H  = WS_O + (size_t)SEQ * DM * 2;
constexpr size_t WS_HN = WS_H + (size_t)SEQ * DM * 2;
constexpr size_t WS_U  = WS_HN + (size_t)SEQ * DM * 2;
constexpr size_t WS_ST = WS_U + (size_t)DM * FF * 2;
constexpr size_t WS_ST_BYTES = (size_t)32 * 8 * 64 * 16;
constexpr size_t WS_END = WS_ST + WS_ST_BYTES;

DI rsrc_t mk_rsrc(const void* p, unsigned bytes) { return __builtin_amdgcn_make_buffer_rsrc((void*)p, (short)0, (int)bytes, 0x00020000); }
DI void dma16(unsigned lds, unsigned voff, rsrc_t rs, unsigned soff) {
    asm volatile("s_mov_b32 m0, %0\n\ts_nop 0\n\tbuffer_load_dwordx4 %1, %2, %3 offen lds" ::"s"(lds), "v"(voff), "s"(rs), "s"(soff) : "memory");
}
template <int N> DI void wait_bar() { asm volatile("s_waitcnt vmcnt(%0) lgkmcnt(0)\n\ts_barrier" ::"i"(N) : "memory"); }
DI void lds_bar() { asm volatile("s_waitcnt lgkmcnt(0)\n\ts_barrier" ::: "memory"); }
DI unsigned cvtpk(float lo, float hi) {
    typedef __attribute__((ext_vector_type(2))) float f32x2;
    return __builtin_bit_cast(unsigned, __builtin_convertvector(f32x2{lo, hi}, bf16x2v));
}
DI float bf_lo(unsigned u) { return __uint_as_float(u << 16); }
DI float bf_hi(unsigned u) { return __uint_as_float(u & 0xffff0000u); }
DI bf16x4 ldtr(const LAS char* p) { return __builtin_amdgcn_ds_read_tr16_b64_v4i16((LAS bf16x4*)p); }
DI bf16x8 ldtr2(const LAS char* p0, const LAS char* p1) {
    const bf16x4 a = ldtr(p0), b = ldtr(p1);
    return __builtin_shufflevector(a, b, 0, 1, 2, 3, 4, 5, 6, 7);
}
DI bf16x8 ld128(const LAS char* p) { return *(const LAS bf16x8*)p; }
DI f32x16 mfma32(bf16x8 a, bf16x8 b, f32x16 c) { return __builtin_amdgcn_mfma_f32_32x32x16_bf16(a, b, c, 0, 0, 0); }
DI f32x16 mfma32k8(bf16x4 a, bf16x4 b, f32x16 c) { return __builtin_amdgcn_mfma_f32_32x32x8bf16_1k(a, b, c, 0, 0, 0); }
DI f32x4 mfma16(bf16x8 a, bf16x8 b, f32x4 c) { return __builtin_amdgcn_mfma_f32_16x16x32_bf16(a, b, c, 0, 0, 0); }
DI float dot2(unsigned a, unsigned b, float c) {
    return __builtin_amdgcn_fdot2_f32_bf16(__builtin_bit_cast(bf16x2v, a), __builtin_bit_cast(bf16x2v, b), c, false);
}
DI float xhalf_sum(float v) {
    auto r = __builtin_amdgcn_permlane32_swap(__float_as_uint(v), __float_as_uint(v), false, false);
    return __uint_as_float(r[0]) + __uint_as_float(r[1]);
}
DI float xhalf_max(float v) {
    auto r = __builtin_amdgcn_permlane32_swap(__float_as_uint(v), __float_as_uint(v), false, false);
    return __builtin_fmaxf(__uint_as_float(r[0]), __uint_as_float(r[1]));
}
DI int uni(int v) { return __builtin_amdgcn_readfirstlane(v); }
#define PIN(x) asm volatile("" : "+v"(x))
#define SBAR() __builtin_amdgcn_sched_barrier(0)
#define ST_AUX 0
DI void st64(rsrc_t rs, unsigned off, u32x2 v) { __builtin_amdgcn_raw_buffer_store_b64(v, rs, off, 0, ST_AUX); }
DI void st128(rsrc_t rs, unsigned off, u32x4 v) { __builtin_amdgcn_raw_buffer_store_b128(v, rs, off, 0, ST_AUX); }
DI u32x2 ld64g(rsrc_t rs, unsigned off) { return __builtin_bit_cast(u32x2, __builtin_amdgcn_raw_buffer_load_b64(rs, off, 0, 0)); }
DI float rbf(float v) { return bf_lo(cvtpk(v, 0.f)); }
DI void ln_scalars(float s, float ss, float& mean, float& rstd) {
    const float m = s * (1.f / DM), var = fmaxf(ss * (1.f / DM) - m * m, 0.f);
    mean = rbf(m);
    rstd = __builtin_amdgcn_rcpf(rbf(__builtin_amdgcn_sqrtf(rbf(rbf(var) + EPS))));
}
#define PROF_ARG
#define PROF_PASS
#define PROF_DECL
#define MARK(ph, i)
#define PROF_END(ph)

template <int NP, int NT, class Issue, class Work> DI void ring_loader(Issue issue, Work work) {
    static_assert(NT >= 5, "ring depth");
    issue(0); issue(1);
    wait_bar<NP>();
    issue(2); issue(3);
#pragma unroll 1
    for (int t = 0; t + 1 < NT; ++t) {
        work(t);
        if (t + 3 < NT) wait_bar<2 * NP>(); else if (t + 2 < NT) wait_bar<NP>(); else wait_bar<0>();
        if (t + 4 < NT) issue(t + 4);
    }
    work(NT - 1);
}
template <int NP, int NT, class Issue> DI void ring_loader(Issue issue) { ring_loader<NP, NT>(issue, [](int) {}); }

struct Params {
    const uint16_t* x; const uint16_t* wqkv; const uint16_t* wo; const uint16_t* w1; const uint16_t* b1; const uint16_t* w2; const uint16_t* b2;
    uint16_t* out; char* ws;
};

constexpr int A_STAGE = 32768, A_NT = 10, A_STAT = A_STAGE * 4;
constexpr int A_LDS = A_STAT + 3 * 128 * 4;

template <bool ISV>
DI void phaseA_body(const Params& p, LAS char* sm, int rb, int ct PROF_ARG) {
    const int tid = threadIdx.x, l = tid & 63, w = uni(tid >> 6), r = l & 31, h = l >> 5;
    const int row0 = rb * 128, col0 = ct * 128;
    const unsigned sm0 = (unsigned)(uintptr_t)sm;
    LAS float* sa = (LAS float*)(sm + A_STAT);
    LAS float* sb = sa + 128;
    LAS float* cst = sa + 256;
    MARK(0, 0);
    if (w >= 4) {
        const bool isX = w < 6;
        const rsrc_t rs = mk_rsrc(isX ? (const void*)p.x : (const void*)p.wqkv, isX ? SEQ * DM * 2 : DM * 3 * DM * 2);
        unsigned voff[2];
#pragma unroll
        for (int j = 0; j < 2; ++j) {
            const int u = (w & 1) * 8 + j;
            const int ra = 8 * u + (l >> 3), ca = (l & 7) ^ ((ra >> 1) & 7);
            const int kb = 4 * u + (l >> 4), cb = (l & 15) ^ ((kb & 3) << 2);
            voff[j] = isX ? (unsigned)((row0 + ra) * (DM * 2) + ca * 16) : (unsigned)(kb * (3 * DM * 2) + col0 * 2 + cb * 16);
        }
        const unsigned kstep = isX ? 128u : 64u * (3 * DM * 2), pstep = isX ? 16u * (DM * 2) : 8u * (3 * DM * 2);
        const unsigned ldst = sm0 + (w - 4) * 8192;
        ring_loader<8, A_NT>([&](int t) {
#pragma unroll
            for (int j = 0; j < 8; ++j) dma16(ldst + (t & 3) * A_STAGE + j * 1024, voff[j & 1], rs, kstep * t + pstep * (j >> 1));
        });
        lds_bar();
        return;
    }
    const int wm = w >> 1, wn = w & 1;
    const int tb0 = 64 * wm + 32 * wn, tb1 = tb0 ^ 32, cb0 = 64 * wn + 32 * wm, cb1 = cb0 ^ 32;
    const int g = l >> 4, q = (l & 15) >> 2, p4 = l & 3;
    unsigned xa[4];
#pragma unroll
    for (int ks = 0; ks < 4; ++ks) xa[ks] = ((tb0 + r) * 128 + ((h ^ ((r >> 1) & 7)) << 4)) ^ (ks << 5);
    const unsigned wa = 16384 + (8 * (g >> 1) + q) * 256 + ((((cb0 >> 3) + 2 * (g & 1) + (p4 >> 1)) ^ (q << 2)) << 4) + (p4 & 1) * 8;
    f32x16 acc[2][2] = {};
    struct Frag { bf16x8 x0, x1, w0, w1; } F0, F1;
    auto rd = [&](Frag& F, const LAS char* st, int ks) {
        F.x0 = ld128(st + xa[ks]);
        F.w0 = ldtr2(st + wa + ks * 4096, st + wa + ks * 4096 + 1024);
        F.x1 = ld128(st + (xa[ks] ^ 4096));
        F.w1 = ldtr2(st + (wa ^ 64) + ks * 4096, st + (wa ^ 64) + ks * 4096 + 1024);
        SBAR();
    };
    auto mf = [&](bf16x8 x, bf16x8 wv, f32x16 c) { return ISV ? mfma32(x, wv, c) : mfma32(wv, x, c); };
    float xs0 = 0.f, xs1 = 0.f, xq0 = 0.f, xq1 = 0.f;
    f32x16 csum = {};
    const bf16x8 ones = __builtin_bit_cast(bf16x8, u32x4{0x3f803f80u, 0x3f803f80u, 0x3f803f80u, 0x3f803f80u});
    auto st2 = [&](unsigned u) {
        float lo, hi;
        asm volatile("v_lshlrev_b32 %0, 16, %6\n\tv_and_b32 %1, 0xffff0000, %6\n\tv_add_f32 %2, %2, %0\n\tv_add_f32 %3, %3, %1\n\tv_fmac_f32 %4, %0, %0\n\tv_fmac_f32 %5, %1, %1"
                     : "=&v"(lo), "=&v"(hi), "+v"(xs0), "+v"(xs1), "+v"(xq0), "+v"(xq1) : "v"(u));
        SBAR();
    };
    auto mm = [&](const Frag& F) {
        const u32x4 xu = __builtin_bit_cast(u32x4, F.x0);
        acc[0][0] = mf(F.x0, F.w0, acc[0][0]); SBAR(); st2(xu[0]);
        acc[1][0] = mf(F.x1, F.w0, acc[1][0]); SBAR(); st2(xu[1]);
        csum = mf(ones, F.w0, csum); SBAR(); st2(xu[2]);
        acc[0][1] = mf(F.x0, F.w1, acc[0][1]); SBAR(); st2(xu[3]);
        acc[1][1] = mf(F.x1, F.w1, acc[1][1]); SBAR();
    };
    lds_bar();
    MARK(0, 1);
    rd(F0, sm, 0);
#pragma unroll
    for (int t = 0; t < A_NT; ++t) {
        const LAS char* st = sm + (t & 3) * A_STAGE;
        rd(F1, st, 1); mm(F0);
        rd(F0, st, 2); mm(F1);
        rd(F1, st, 3); mm(F0);
        if (t + 1 < A_NT) { lds_bar(); rd(F0, sm + ((t + 1) & 3) * A_STAGE, 0); }
        mm(F1);
    }
    PIN(csum);
    MARK(0, 2);
    {
        float mean, ra;
        ln_scalars(xhalf_sum(xs0 + xs1), xhalf_sum(xq0 + xq1), mean, ra);
        if (!ISV && ct < 5) ra *= QSCALE;
        if (h == 0) { sa[tb0 + r] = ra; sb[tb0 + r] = -mean * ra; }
        if (ISV) { if (h == 0) cst[cb0 + r] = csum[0]; }
        else if (r == 0) {
#pragma unroll
            for (int gg = 0; gg < 4; ++gg) *(LAS f32x4*)(cst + cb0 + 8 * gg + 4 * h) = f32x4{csum[4 * gg], csum[4 * gg + 1], csum[4 * gg + 2], csum[4 * gg + 3]};
        }
    }
    lds_bar();
    const rsrc_t rw = mk_rsrc(p.ws, (unsigned)WS_END);
    if (ISV) {
#pragma unroll
        for (int a = 0; a < 2; ++a) {
            const int tb = a ? tb1 : tb0, T = rb * 4 + (tb >> 5);
            f32x4 av[4], bv[4];
#pragma unroll
            for (int gg = 0; gg < 4; ++gg) { av[gg] = *(const LAS f32x4*)(sa + tb + 8 * gg + 4 * h); bv[gg] = *(const LAS f32x4*)(sb + tb + 8 * gg + 4 * h); }
#pragma unroll
            for (int j = 0; j < 2; ++j) {
                const int cl = (j ? cb1 : cb0) + r;
                const float cs = cst[cl];
                const int c = col0 + cl - 2 * DM, head = c / 20, d = c - head * 20;
                const unsigned dst = (unsigned)WS_KV + (head * 64 + T) * 3072 + 1536 + h * 384 + d * 16;
#pragma unroll
                for (int s = 0; s < 2; ++s) {
                    u32x4 v;
#pragma unroll
                    for (int i = 0; i < 4; ++i) {
                        const int e = 8 * s + 2 * i;
                        v[i] = cvtpk(av[e >> 2][e & 3] * acc[a][j][e] + bv[e >> 2][e & 3] * cs, av[e >> 2][(e & 3) + 1] * acc[a][j][e + 1] + bv[e >> 2][(e & 3) + 1] * cs);
                    }
                    st128(rw, dst + s * 768, v);
                    if (d >= 16) {
                        const unsigned c1 = d == 16 ? 0x3f803f80u : 0u;
                        st128(rw, dst + s * 768 + 64, u32x4{c1, c1, c1, c1});
                    }
                }
            }
        }
    } else {
        const bool isQ = ct < 5;
        const float ra0 = sa[tb0 + r], rb0 = sb[tb0 + r], ra1 = sa[tb1 + r], rb1 = sb[tb1 + r];
#pragma unroll
        for (int j = 0; j < 2; ++j)
#pragma unroll
            for (int gg = 0; gg < 4; ++gg) {
                const int cl = (j ? cb1 : cb0) + 8 * gg + 4 * h;
                const f32x4 cs = *(const LAS f32x4*)(cst + cl);
                const int c4 = (col0 + cl - (isQ ? 0 : DM)) >> 2, head = c4 / 5, grp = c4 - head * 5;
                const unsigned in0 = ((grp >> 1) * 32 + r) * 16 + (grp & 1) * 8;
#pragma unroll
                for (int a = 0; a < 2; ++a) {
                    const float ra = a ? ra1 : ra0, rbb = a ? rb1 : rb0;
                    const int T = rb * 4 + ((a ? tb1 : tb0) >> 5);
                    u32x2 v;
                    v[0] = cvtpk(ra * acc[a][j][4 * gg] + rbb * cs[0], ra * acc[a][j][4 * gg + 1] + rbb * cs[1]);
                    v[1] = cvtpk(ra * acc[a][j][4 * gg + 2] + rbb * cs[2], ra * acc[a][j][4 * gg + 3] + rbb * cs[3]);
                    if (isQ) {
                        st64(rw, grp < 4 ? (unsigned)WS_Q0 + (head * 64 + T) * 1024 + in0 : (unsigned)WS_Q1 + (head * SEQ + T * 32 + r) * 8, v);
                    } else {
                        const unsigned base = (unsigned)WS_KV + (head * 64 + T) * 3072;
                        st64(rw, base + (grp < 4 ? in0 : 1024 + r * 8), v);
                        if (grp == 4) st64(rw, base + 1280 + r * 8, u32x2{0x3f803f80u, 0x00003f80u});
                    }
                }
            }
    }
}
DI void phaseA(const Params& p, LAS char* sm, int b PROF_ARG) {
    const int g = b & 7, i = b >> 3, rb = 4 * (g & 3) + (i & 3), ct = (g >> 2) * 8 + (i >> 2);
    if (ct >= 15) return;
    if (ct >= 10) phaseA_body<true>(p, sm, rb, ct PROF_PASS); else phaseA_body<false>(p, sm, rb, ct PROF_PASS);
    MARK(0, 3);
}

constexpr int B_STAGE = 8 * 3072, B_NS = 3, B_NT = 8, B_FLAG = B_STAGE * B_NS;
constexpr int B_LDS = B_FLAG + 64;

DI u32x2 neg_split3(float m) {
    const unsigned hi = cvtpk(m, 0.f) & 0xffffu; const float r1 = m - bf_lo(hi);
    const unsigned mid = cvtpk(r1, 0.f) & 0xffffu; const float r2 = r1 - bf_lo(mid);
    const unsigned lo = cvtpk(r2, 0.f) & 0xffffu;
    return u32x2{(hi | (mid << 16)) ^ 0x80008000u, lo ^ 0x8000u};
}
DI float max16(const f32x16& s) {
    float a = __builtin_fmaxf(__builtin_fmaxf(s[0], s[1]), s[2]), b = __builtin_fmaxf(__builtin_fmaxf(s[3], s[4]), s[5]);
    a = __builtin_fmaxf(__builtin_fmaxf(a, s[6]), s[7]); b = __builtin_fmaxf(__builtin_fmaxf(b, s[8]), s[9]);
    a = __builtin_fmaxf(__builtin_fmaxf(a, s[10]), s[11]); b = __builtin_fmaxf(__builtin_fmaxf(b, s[12]), s[13]);
    a = __builtin_fmaxf(__builtin_fmaxf(a, s[14]), s[15]);
    return __builtin_fmaxf(a, b);
}
DI bf16x4 ld64(const LAS char* p) { return *(const LAS bf16x4*)p; }

DI void phaseB(const Params& p, LAS char* sm, int b PROF_ARG) {
    const int tid = threadIdx.x, l = tid & 63, w = uni(tid >> 6), r = l & 31, h = l >> 5;
    const int g = b & 7, i = b >> 3, head = 4 * g + (i >> 3);
    const int kh = w >> 2, TA = (i & 7) * 8 + (w & 3) * 2;
    const unsigned sm0 = (unsigned)(uintptr_t)sm;
    const rsrc_t rs = mk_rsrc(p.ws + WS_KV + (size_t)head * 64 * 3072, 64 * 3072);
    const unsigned voff = (kh * 32 + (w & 3)) * 3072 + l * 16;
    auto issue = [&](int t, int slot) {
#pragma unroll
        for (int j = 0; j < 3; ++j) dma16(sm0 + slot * B_STAGE + w * 3072 + j * 1024, voff, rs, t * 4 * 3072 + j * 1024);
    };
    MARK(1, 0);
    issue(0, 0); issue(1, 1);
    const char* qp0 = p.ws + WS_Q0 + ((size_t)(head * 64 + TA) * 64 + l) * 16;
    const char* qp1 = p.ws + WS_Q1 + ((size_t)head * SEQ + TA * 32 + r) * 8;
    bf16x8 qA0 = *(const bf16x8*)qp0, qB0 = *(const bf16x8*)(qp0 + 1024);
    u32x2 qA1u = *(const u32x2*)qp1, qB1u = *(const u32x2*)(qp1 + 256);
    PIN(qA0); PIN(qB0); PIN(qA1u); PIN(qB1u);
    if (h) { qA1u = u32x2{0u, 0u}; qB1u = u32x2{0u, 0u}; }
    const unsigned k0o = l * 16, k1o = 1024 + l * 8, vo = 1536 + h * 384 + (r < 22 ? r : 22) * 16;
    const LAS char* sw = sm + kh * 4 * 3072;
    f32x16 oA, oB;
    float lsum, mA, mB;
    auto scores = [&](const LAS char* tp, bf16x8 q0, u32x2 q1) {
        return mfma32k8(ld64(tp + k1o), __builtin_bit_cast(bf16x4, q1), mfma32(ld128(tp + k0o), q0, f32x16{}));
    };
    for (int pass = 0;; ++pass) {
        if (pass == 1) {
            if (h) { qA1u = u32x2{0u, 0u}; qB1u = u32x2{0u, 0u}; }
            mA = -INFINITY; mB = -INFINITY;
            issue(0, 0); issue(1, 1);
            for (int t = 0; t < B_NT; ++t) {
                if (t + 1 < B_NT) wait_bar<3>(); else wait_bar<0>();
                if (t + 2 < B_NT) issue(t + 2, (t + 2) % B_NS);
                const LAS char* st = sm + (t % B_NS) * B_STAGE;
#pragma unroll 1
                for (int kt = 0; kt < 8; ++kt) {
                    mA = __builtin_fmaxf(mA, max16(scores(st + kt * 3072, qA0, qA1u)));
                    mB = __builtin_fmaxf(mB, max16(scores(st + kt * 3072, qB0, qB1u)));
                }
            }
            mA = xhalf_max(mA); mB = xhalf_max(mB);
            lds_bar();
            if (h) { qA1u = neg_split3(mA); qB1u = neg_split3(mB); }
            issue(0, 0); issue(1, 1);
        }
        wait_bar<3>();
        if (pass == 0) {
            const f32x16 s0 = scores(sw, qA0, qA1u), s1 = scores(sw, qB0, qB1u);
            mA = xhalf_max(max16(s0)); mB = xhalf_max(max16(s1));
            if (h) { qA1u = neg_split3(mA); qB1u = neg_split3(mB); }
        }
        MARK(1, 1);
        const bf16x4 qA1 = __builtin_bit_cast(bf16x4, qA1u), qB1 = __builtin_bit_cast(bf16x4, qB1u);
        oA = f32x16{}; oB = f32x16{};
        bf16x8 kf0 = ld128(sw + k0o), vfa = ld128(sw + vo), vfb, vf1 = bf16x8{};
        bf16x4 kf1 = ld64(sw + k1o);
        f32x16 sA = mfma32k8(kf1, qA1, mfma32(kf0, qA0, f32x16{})), sB;
        u32x4 p0, p1 = u32x4{0u, 0u, 0u, 0u};
        int slot = 0;
#define EX(v) __builtin_amdgcn_exp2f(v)
#define BF(P) __builtin_bit_cast(bf16x8, P)
#define GRP(S, a, P, c) do { \
            const float e0_ = EX(S[a]), e1_ = EX(S[a + 1]), e2_ = EX(S[a + 2]), e3_ = EX(S[a + 3]); \
            P[c] = cvtpk(e0_, e1_); P[c + 1] = cvtpk(e2_, e3_); PIN(P); SBAR(); } while (0)
#define MF(stmt) do { stmt; SBAR(); } while (0)
#define TILE(TC, TN, V0, V0N) do { \
            MF(sB = mfma32(kf0, qB0, f32x16{}));          GRP(sA, 0, p0, 0); \
            MF(oB = mfma32(vf1, BF(p1), oB)); \
            MF((kf0 = ld128((TN) + k0o), vf1 = ld128((TC) + vo + 768), V0N = ld128((TN) + vo))); \
                                                          GRP(sA, 4, p0, 2); \
            MF(sB = mfma32k8(kf1, qB1, sB)); \
            MF(kf1 = ld64((TN) + k1o));                   GRP(sA, 8, p1, 0); \
            MF(oA = mfma32(V0, BF(p0), oA));              GRP(sA, 12, p1, 2); \
            asm volatile("" : "+v"(kf0), "+v"(kf1), "+v"(vf1), "+v"(V0N)); SBAR(); \
            MF(sA = mfma32(kf0, qA0, f32x16{}));          GRP(sB, 0, p0, 0); \
            MF(oA = mfma32(vf1, BF(p1), oA));             GRP(sB, 4, p0, 2); \
            MF(sA = mfma32k8(kf1, qA1, sA));              GRP(sB, 8, p1, 0); \
            MF(oB = mfma32(V0, BF(p0), oB));              GRP(sB, 12, p1, 2); } while (0)
#pragma unroll 1
        for (int t = 0; t < B_NT; ++t) {
            const int nslot = slot == 2 ? 0 : slot + 1;
            const LAS char* st = sw + slot * B_STAGE;
            const LAS char* sn = sw + nslot * B_STAGE;
            TILE(st, st + 1 * 3072, vfa, vfb);
            TILE(st + 1 * 3072, st + 2 * 3072, vfb, vfa);
            TILE(st + 2 * 3072, st + 3 * 3072, vfa, vfb);
            wait_bar<0>();
            if (t + 2 < B_NT) issue(t + 2, nslot == 2 ? 0 : nslot + 1);
            TILE(st + 3 * 3072, sn, vfb, vfa);
            slot = nslot;
        }
        oB = mfma32(vf1, BF(p1), oB);
#undef TILE
#undef MF
#undef GRP
#undef BF
#undef EX
        MARK(1, 2);
        lds_bar();
        LAS f32x4* red = (LAS f32x4*)sm + (w & 3) * 7 * 64 + l;
        LAS float* mex = (LAS float*)((LAS f32x4*)sm + ((w & 3) * 7 + 6) * 64);
        f32x16 o = kh ? oB : oA;
        {
            const f32x16 give = kh ? oA : oB;
#pragma unroll
            for (int e = 0; e < 3; ++e) red[(kh * 3 + e) * 64] = f32x4{give[4 * e], give[4 * e + 1], give[4 * e + 2], give[4 * e + 3]};
            mex[kh * 64 + l] = kh ? mA : mB;
        }
        lds_bar();
        {
            const float mo = mex[(kh ^ 1) * 64 + l], mm = kh ? mB : mA, mx = __builtin_fmaxf(mo, mm);
            const float fo = __builtin_amdgcn_exp2f(mo - mx), fm = __builtin_amdgcn_exp2f(mm - mx);
#pragma unroll
            for (int e = 0; e < 3; ++e) {
                const f32x4 v = red[((kh ^ 1) * 3 + e) * 64];
#pragma unroll
                for (int k = 0; k < 4; ++k) o[4 * e + k] = o[4 * e + k] * fm + v[k] * fo;
            }
        }
        oA = o;
        {
            auto rr = __builtin_amdgcn_permlane32_swap(__float_as_uint(o[8]), __float_as_uint(o[8]), false, false);
            lsum = __uint_as_float(rr[1]);
        }
        if (pass == 1) break;
        bool ok = lsum > 0x1p-30f && lsum < 0x1p64f;
#pragma unroll
        for (int e = 0; e < 12; ++e) ok = ok && (e >= 8 && h ? true : __builtin_fabsf(o[e]) < INFINITY);
        LAS int* flag = (LAS int*)(sm + B_FLAG);
        const bool wave_ok = __all(ok);
        if (l == 0) flag[w] = wave_ok ? 0 : 1;
        lds_bar();
        const u32x4 f0 = *(LAS u32x4*)flag, f1 = *(LAS u32x4*)(flag + 4);
        if (!uni(f0[0] | f0[1] | f0[2] | f0[3] | f1[0] | f1[1] | f1[2] | f1[3])) break;
        lds_bar();
    }
    const float inv = __builtin_amdgcn_rcpf(lsum);
    const rsrc_t rw = mk_rsrc(p.ws, (unsigned)WS_END);
    const unsigned orow = (unsigned)WS_O + ((TA + kh) * 32 + r) * (DM * 2);
#pragma unroll
    for (int e = 0; e < 3; ++e) {
        if (e < 2 || h == 0) {
            const int gam = head * 5 + 2 * e + h;
            const int G = gam & 7, pos = (gam & ~7) + 2 * (G & 3) + (G >> 2);
            u32x2 v;
            v[0] = cvtpk(oA[4 * e] * inv, oA[4 * e + 1] * inv); v[1] = cvtpk(oA[4 * e + 2] * inv, oA[4 * e + 3] * inv);
            st64(rw, orow + pos * 8, v);
        }
    }
    MARK(1, 3);
}

constexpr int C_STAGE = 36864, C_NT = 5, C_RED = C_STAGE * 4;
constexpr int C_LDS = C_RED + 8 * 64 * 8 + 64;

DI void phaseC(const Params& p, LAS char* sm, int b PROF_ARG) {
    const int tid = threadIdx.x, l = tid & 63, w = uni(tid >> 6);
    const int gx = b & 7, ix = b >> 3, rb = 4 * gx + (ix >> 3), cb = ix & 7;
    const int row0 = rb * 64, col0 = cb * 80;
    const unsigned sm0 = (unsigned)(uintptr_t)sm;
    const int tk = l & 15, g = l >> 4, q = (l & 15) >> 2, p4 = l & 3;
    const int row = row0 + 16 * (w & 3) + tk;
    const rsrc_t rw = mk_rsrc(p.ws, (unsigned)WS_END);
    float hv[20];
    unsigned long long* gran = (unsigned long long*)(p.ws + WS_ST) + (size_t)rb * 8 * 64 * 2;
    MARK(2, 0);
    if (w >= 4) {
        const int lw = w - 4;
        const rsrc_t rsO = mk_rsrc(p.ws + WS_O, SEQ * DM * 2), rsW = mk_rsrc(p.wo, DM * DM * 2);
        unsigned vo[4], vw[5];
#pragma unroll
        for (int j = 0; j < 4; ++j) { const int rr = 4 * (lw * 4 + j) + (l >> 4), c = (l & 15) ^ (rr & 15); vo[j] = (row0 + rr) * (DM * 2) + c * 16; }
#pragma unroll
        for (int j = 0; j < 5; ++j) { const int ch = (lw * 5 + j) * 64 + l, kr = ch / 10, c = ch - kr * 10; vw[j] = kr * (DM * 2) + col0 * 2 + c * 16; }
        ring_loader<9, C_NT>([&](int t) {
            const unsigned base = sm0 + (t & 3) * C_STAGE;
#pragma unroll
            for (int j = 0; j < 4; ++j) dma16(base + (lw * 4 + j) * 1024, vo[j], rsO, t * 256);
#pragma unroll
            for (int j = 0; j < 5; ++j) dma16(base + 16384 + (lw * 5 + j) * 1024, vw[j], rsW, t * 128 * (DM * 2));
        });
    } else {
        u32x2 xv[5];
        {
            const rsrc_t rx = mk_rsrc(p.x, SEQ * DM * 2);
#pragma unroll
            for (int j = 0; j < 5; ++j) xv[j] = ld64g(rx, (row * DM + col0 + 16 * j + 4 * g) * 2);
        }
        unsigned oa[4];
#pragma unroll
        for (int ks = 0; ks < 4; ++ks) oa[ks] = (16 * w + tk) * 256 + (((4 * ks + g) ^ tk) << 4);
        const unsigned wa = 16384 + (4 * g + q) * 160 + p4 * 8;
        f32x4 acc[5] = {};
        struct Frag { bf16x8 o, w[5]; } F0, F1;
        auto rd = [&](Frag& F, const LAS char* st, int ks) {
            F.o = ld128(st + oa[ks]);
#pragma unroll
            for (int j = 0; j < 5; ++j) F.w[j] = ldtr2(st + wa + ks * 32 * 160 + j * 32, st + wa + ks * 32 * 160 + j * 32 + 16 * 160);
            SBAR();
        };
        auto mm = [&](const Frag& F) {
#pragma unroll
            for (int j = 0; j < 5; ++j) acc[j] = mfma16(F.w[j], F.o, acc[j]);
            SBAR();
        };
        lds_bar();
        MARK(2, 1);
        rd(F0, sm, 0);
#pragma unroll
        for (int t = 0; t < C_NT; ++t) {
            const LAS char* st = sm + (t & 3) * C_STAGE;
            rd(F1, st, 1); mm(F0);
            rd(F0, st, 2); mm(F1);
            rd(F1, st, 3); mm(F0);
            if (t + 1 < C_NT) { lds_bar(); rd(F0, sm + ((t + 1) & 3) * C_STAGE, 0); }
            mm(F1);
        }
        MARK(2, 2);
        float s = 0.f, ss = 0.f;
        u32x2 hb[5];
#pragma unroll
        for (int j = 0; j < 5; ++j) {
            const f32x4 a = acc[j];
            hb[j][0] = cvtpk(a[0] + bf_lo(xv[j][0]), a[1] + bf_hi(xv[j][0])); hb[j][1] = cvtpk(a[2] + bf_lo(xv[j][1]), a[3] + bf_hi(xv[j][1]));
            hv[4 * j] = bf_lo(hb[j][0]); hv[4 * j + 1] = bf_hi(hb[j][0]); hv[4 * j + 2] = bf_lo(hb[j][1]); hv[4 * j + 3] = bf_hi(hb[j][1]);
#pragma unroll
            for (int e = 0; e < 4; ++e) { s += hv[4 * j + e]; ss += hv[4 * j + e] * hv[4 * j + e]; }
        }
        s += __shfl_xor(s, 16); ss += __shfl_xor(ss, 16); s = xhalf_sum(s); ss = xhalf_sum(ss);
        if (g < 2) {
            const unsigned long long v = (1ull << 32) | __float_as_uint(g == 0 ? s : ss);
            __hip_atomic_store(gran + ((size_t)cb * 64 + 16 * w + tk) * 2 + g, v, __ATOMIC_RELAXED, __HIP_MEMORY_SCOPE_AGENT);
        }
#pragma unroll
        for (int j = 0; j < 5; ++j) st64(rw, (unsigned)WS_H + (row * DM + col0 + 16 * j + 4 * g) * 2, hb[j]);
    }
    MARK(2, 3);
    LAS float* fin = (LAS float*)(sm + C_RED);
    {
        unsigned long long a, c; unsigned spins = 0;
        for (;;) {
            a = __hip_atomic_load(gran + ((size_t)w * 64 + l) * 2, __ATOMIC_RELAXED, __HIP_MEMORY_SCOPE_AGENT);
            c = __hip_atomic_load(gran + ((size_t)w * 64 + l) * 2 + 1, __ATOMIC_RELAXED, __HIP_MEMORY_SCOPE_AGENT);
            if (__all((a >> 32) == 1ull && (c >> 32) == 1ull)) break;
            if (++spins > (1u << 20)) break;
            __builtin_amdgcn_s_sleep(1);
        }
        *(LAS u32x2*)(fin + (w * 64 + l) * 2) = u32x2{(unsigned)a, (unsigned)c};
    }
    lds_bar();
    MARK(2, 4);
    if (w < 4) {
        float s = 0.f, ss = 0.f;
#pragma unroll
        for (int j = 0; j < 8; ++j) { const u32x2 v = *(const LAS u32x2*)(fin + (j * 64 + 16 * w + tk) * 2); s += __uint_as_float(v[0]); ss += __uint_as_float(v[1]); }
        const float m = s * (1.f / DM), var = fmaxf(ss * (1.f / DM) - m * m, 0.f);
        const float r1 = __builtin_amdgcn_rsqf(var + EPS);
        const float r2 = __builtin_amdgcn_rsqf(var * r1 * r1 + EPS), rr = r1 * r2;
#pragma unroll
        for (int j = 0; j < 5; ++j) {
            u32x2 v;
            v[0] = cvtpk((hv[4 * j] - m) * rr, (hv[4 * j + 1] - m) * rr); v[1] = cvtpk((hv[4 * j + 2] - m) * rr, (hv[4 * j + 3] - m) * rr);
            st64(rw, (unsigned)WS_HN + (row * DM + col0 + 16 * j + 4 * g) * 2, v);
        }
    }
    MARK(2, 5);
}

constexpr int D_STAGE = 32768, D_NT = 16;
constexpr int D_LDS = D_STAGE * 4;

DI float gelu_tanh(float v) {
    const float y = v * (0.7978845608028654f + 0.7978845608028654f * 0.044715f * v * v);
    return v * __builtin_amdgcn_rcpf(1.f + __builtin_amdgcn_exp2f(-2.f * 1.4426950408889634f * y));
}
DI void phaseD(const Params& p, LAS char* sm, int b PROF_ARG) {
    const int gx = b & 7, ix = b >> 3;
    if (ix >= 30) return;
    const int db = 5 * (gx & 1) + ix % 5, fb = 6 * (gx >> 1) + ix / 5;
    const int tid = threadIdx.x, l = tid & 63, w = uni(tid >> 6);
    const unsigned sm0 = (unsigned)(uintptr_t)sm;
    MARK(3, 0);
    if (w >= 4) {
        const bool isH = w < 6;
        const int pitch = isH ? DM * 2 : FF * 2;
        const rsrc_t rs = mk_rsrc(isH ? (const void*)(p.ws + WS_HN) : (const void*)p.w1, isH ? SEQ * DM * 2 : SEQ * FF * 2);
        const int kr = 64 * (w & 1) + (l >> 3);
        const unsigned voff = kr * pitch + (isH ? db : fb) * 128 + (((l & 7) ^ (((kr >> 1) & 1) << 2)) << 4);
        const unsigned ldst = sm0 + (w - 4) * 8192;
        ring_loader<8, D_NT>([&](int t) {
#pragma unroll
            for (int j = 0; j < 8; ++j) dma16(ldst + (t & 3) * D_STAGE + j * 1024, voff, rs, (unsigned)((t * 128 + j * 8) * pitch));
        });
        return;
    }
    const int wf = w >> 1, wd = w & 1, r = l & 31, h = l >> 5;
    const int g = l >> 4, q = (l & 15) >> 2, p4 = l & 3;
    u32x2 bv[4];
    {
        const rsrc_t rb1 = mk_rsrc(p.b1, FF * 2);
#pragma unroll
        for (int e = 0; e < 4; ++e) bv[e] = ld64g(rb1, (fb * 64 + 32 * wf + 8 * e + 4 * h) * 2);
    }
    const unsigned rowo = (8 * (g >> 1) + q) * 128 + (p4 & 1) * 8, sw = ((q >> 1) & 1) << 2;
    const unsigned ha = rowo + (((4 * wd + 2 * (g & 1) + (p4 >> 1)) ^ sw) << 4);
    const unsigned wa = 16384 + rowo + (((4 * wf + 2 * (g & 1) + (p4 >> 1)) ^ sw) << 4);
    f32x16 acc = {};
    struct Frag { bf16x8 h, w; } F0, F1;
    auto rd = [&](Frag& F, const LAS char* st, int ks) {
        F.h = ldtr2(st + ha + ks * 2048, st + ha + ks * 2048 + 512);
        F.w = ldtr2(st + wa + ks * 2048, st + wa + ks * 2048 + 512);
        SBAR();
    };
    auto mm = [&](const Frag& F) { acc = mfma32(F.w, F.h, acc); SBAR(); };
    lds_bar();
    MARK(3, 1);
    rd(F0, sm, 0);
#pragma unroll
    for (int t = 0; t < D_NT; ++t) {
        const LAS char* st = sm + (t & 3) * D_STAGE;
        rd(F1, st, 1); mm(F0);
        rd(F0, st, 2); mm(F1);
        rd(F1, st, 3); mm(F0);
        rd(F0, st, 4); mm(F1);
        rd(F1, st, 5); mm(F0);
        rd(F0, st, 6); mm(F1);
        rd(F1, st, 7); mm(F0);
        if (t + 1 < D_NT) { lds_bar(); rd(F0, sm + ((t + 1) & 3) * D_STAGE, 0); }
        mm(F1);
    }
    MARK(3, 2);
    {
        const rsrc_t rw = mk_rsrc(p.ws, (unsigned)WS_END);
        const int d = db * 64 + 32 * wd + r;
#pragma unroll
        for (int e = 0; e < 4; ++e) {
            const int f0 = fb * 64 + 32 * wf + 8 * e + 4 * h;
            u32x2 v;
            v[0] = cvtpk(gelu_tanh(acc[4 * e] + bf_lo(bv[e][0])), gelu_tanh(acc[4 * e + 1] + bf_hi(bv[e][0])));
            v[1] = cvtpk(gelu_tanh(acc[4 * e + 2] + bf_lo(bv[e][1])), gelu_tanh(acc[4 * e + 3] + bf_hi(bv[e][1])));
            st64(rw, (unsigned)WS_U + (d * FF + f0) * 2, v);
        }
    }
    MARK(3, 3);
}

constexpr int E_STAGE = 36864, E_NT = 12;
constexpr int E_LDS = E_STAGE * 4;

DI void phaseE(const Params& p, LAS char* sm, int b PROF_ARG) {
    const int gx = b & 7, ix = b >> 3, db = 4 * (gx & 1) + (ix & 3), sb = 8 * (gx >> 1) + (ix >> 2);
    const int d0 = db * 80, s0 = sb * 64;
    const int tid = threadIdx.x, l = tid & 63, w = uni(tid >> 6);
    const unsigned sm0 = (unsigned)(uintptr_t)sm;
    MARK(4, 0);
    if (w >= 4) {
        const int lw = w - 4;
        const rsrc_t rsU = mk_rsrc(p.ws + WS_U, DM * FF * 2), rsW = mk_rsrc(p.w2, FF * SEQ * 2);
        unsigned vu[5], vw[2];
#pragma unroll
        for (int j = 0; j < 5; ++j) { const int rr = 4 * (lw * 5 + j) + (l >> 4), c = (l & 15) ^ (rr & 15); vu[j] = (d0 + rr) * (FF * 2) + c * 16; }
#pragma unroll
        for (int j = 0; j < 2; ++j) {
            const int kr = 8 * (lw * 4 + j) + (l >> 3), fr = ((kr >> 1) & 1) | (((kr >> 3) & 1) << 1);
            vw[j] = kr * (SEQ * 2) + s0 * 2 + (((l & 7) ^ (fr << 1)) << 4);
        }
        ring_loader<9, E_NT>([&](int t) {
            const unsigned base = sm0 + (t & 3) * E_STAGE;
#pragma unroll
            for (int j = 0; j < 5; ++j) dma16(base + (lw * 5 + j) * 1024, vu[j], rsU, t * 256);
#pragma unroll
            for (int j = 0; j < 4; ++j) dma16(base + 20480 + (lw * 4 + j) * 1024, vw[j & 1], rsW, (unsigned)((t * 128 + (j >> 1) * 16) * (SEQ * 2)));
        });
        return;
    }
    const int tk = l & 15, g = l >> 4, q = (l & 15) >> 2, p4 = l & 3;
    const int srow = s0 + 16 * w + tk;
    u32x2 hv[5];
    unsigned b2u;
    {
        const rsrc_t rh = mk_rsrc(p.ws + WS_H, SEQ * DM * 2), rb2 = mk_rsrc(p.b2, SEQ * 2);
#pragma unroll
        for (int j = 0; j < 5; ++j) hv[j] = ld64g(rh, (srow * DM + d0 + 16 * j + 4 * g) * 2);
        b2u = __builtin_amdgcn_raw_buffer_load_b16(rb2, srow * 2, 0, 0);
    }
    const int fr = ((q >> 1) & 1) | ((g & 1) << 1);
    const unsigned wa = 20480 + (8 * g + q) * 128 + (((2 * w + (p4 >> 1)) ^ (fr << 1)) << 4) + (p4 & 1) * 8;
    unsigned uo[4];
#pragma unroll
    for (int ks = 0; ks < 4; ++ks) uo[ks] = tk * 256 + (((4 * ks + g) ^ tk) << 4);
    f32x4 acc[5] = {};
    struct Frag { bf16x8 w, u[5]; } F0, F1;
    auto rd = [&](Frag& F, const LAS char* st, int ks) {
        F.w = ldtr2(st + wa + ks * 4096, st + wa + ks * 4096 + 512);
#pragma unroll
        for (int j = 0; j < 5; ++j) F.u[j] = ld128(st + uo[ks] + j * 4096);
        SBAR();
    };
    auto mm = [&](const Frag& F) {
#pragma unroll
        for (int j = 0; j < 5; ++j) acc[j] = mfma16(F.u[j], F.w, acc[j]);
        SBAR();
    };
    lds_bar();
    MARK(4, 1);
    rd(F0, sm, 0);
#pragma unroll
    for (int t = 0; t < E_NT; ++t) {
        const LAS char* st = sm + (t & 3) * E_STAGE;
        rd(F1, st, 1); mm(F0);
        rd(F0, st, 2); mm(F1);
        rd(F1, st, 3); mm(F0);
        if (t + 1 < E_NT) { lds_bar(); rd(F0, sm + ((t + 1) & 3) * E_STAGE, 0); }
        mm(F1);
    }
    MARK(4, 2);
    {
        const rsrc_t ro = mk_rsrc(p.out, SEQ * DM * 2);
        const float bias = bf_lo(b2u);
#pragma unroll
        for (int j = 0; j < 5; ++j) {
            const f32x4 a = acc[j];
            u32x2 v;
            v[0] = cvtpk(bf_lo(hv[j][0]) + (a[0] + bias), bf_hi(hv[j][0]) + (a[1] + bias));
            v[1] = cvtpk(bf_lo(hv[j][1]) + (a[2] + bias), bf_hi(hv[j][1]) + (a[3] + bias));
            __builtin_amdgcn_raw_buffer_store_b64(v, ro, (srow * DM + d0 + 16 * j + 4 * g) * 2, 0, 0);
        }
    }
    MARK(4, 3);
}

__global__ void __launch_bounds__(512, 2) k_phaseA(Params p) {
    extern __shared__ __attribute__((aligned(16))) char smem[];
    { const int i = blockIdx.x * 512 + threadIdx.x; if (i < (int)(WS_ST_BYTES / 16)) *(u32x4*)(p.ws + WS_ST + (size_t)i * 16) = u32x4{0u, 0u, 0u, 0u}; }
    PROF_DECL phaseA(p, (LAS char*)smem, blockIdx.x PROF_PASS); PROF_END(0);
}
__global__ void __launch_bounds__(512, 2) k_phaseB(Params p) { extern __shared__ __attribute__((aligned(16))) char smem[]; PROF_DECL phaseB(p, (LAS char*)smem, blockIdx.x PROF_PASS); PROF_END(1); }
__global__ void __launch_bounds__(512, 2) k_phaseC(Params p) { extern __shared__ __attribute__((aligned(16))) char smem[]; PROF_DECL phaseC(p, (LAS char*)smem, blockIdx.x PROF_PASS); PROF_END(2); }
__global__ void __launch_bounds__(512, 2) k_phaseD(Params p) { extern __shared__ __attribute__((aligned(16))) char smem[]; PROF_DECL phaseD(p, (LAS char*)smem, blockIdx.x PROF_PASS); PROF_END(3); }
__global__ void __launch_bounds__(512, 2) k_phaseE(Params p) { extern __shared__ __attribute__((aligned(16))) char smem[]; PROF_DECL phaseE(p, (LAS char*)smem, blockIdx.x PROF_PASS); PROF_END(4); }

extern "C" void kernel_launch(void* const* d_in, const int* in_sizes, int n_in, void* d_out, int out_size, void* d_ws, size_t ws_size,
                              hipStream_t stream) {
    Params p;
    p.x = (const uint16_t*)d_in[0]; p.wqkv = (const uint16_t*)d_in[1]; p.wo = (const uint16_t*)d_in[2];
    p.w1 = (const uint16_t*)d_in[4]; p.b1 = (const uint16_t*)d_in[5]; p.w2 = (const uint16_t*)d_in[6]; p.b2 = (const uint16_t*)d_in[7];
    p.out = (uint16_t*)d_out; p.ws = (char*)d_ws;
    (void)hipFuncSetAttribute((const void*)k_phaseA, hipFuncAttributeMaxDynamicSharedMemorySize, A_LDS);
    (void)hipFuncSetAttribute((const void*)k_phaseB, hipFuncAttributeMaxDynamicSharedMemorySize, B_LDS);
    (void)hipFuncSetAttribute((const void*)k_phaseC, hipFuncAttributeMaxDynamicSharedMemorySize, C_LDS);
    (void)hipFuncSetAttribute((const void*)k_phaseD, hipFuncAttributeMaxDynamicSharedMemorySize, D_LDS);
    (void)hipFuncSetAttribute((const void*)k_phaseE, hipFuncAttributeMaxDynamicSharedMemorySize, E_LDS);
    k_phaseA<<<256, 512, A_LDS, stream>>>(p);
    k_phaseB<<<256, 512, B_LDS, stream>>>(p);
    k_phaseC<<<256, 512, C_LDS, stream>>>(p);
    k_phaseD<<<256, 512, D_LDS, stream>>>(p);
    k_phaseE<<<256, 512, E_LDS, stream>>>(p);
}
```

```cpp
#include <hip/hip_runtime.h>
#include <hip/hip_bf16.h>
#include <stdint.h>

#define LAS __attribute__((address_space(3)))
#define DI __device__ __forceinline__
typedef __attribute__((ext_vector_type(8))) short bf16x8;
typedef __attribute__((ext_vector_type(4))) short bf16x4;
typedef __attribute__((ext_vector_type(2))) __bf16 bf16x2v;
typedef __attribute__((ext_vector_type(16))) float f32x16;
typedef __attribute__((ext_vector_type(4))) float f32x4;
typedef __attribute__((ext_vector_type(4))) unsigned u32x4;
typedef __attribute__((ext_vector_type(2))) unsigned u32x2;
typedef __amdgpu_buffer_rsrc_t rsrc_t;

constexpr int SEQ = 2048, DM = 640, NH = 32, FF = 1536;
constexpr float EPS = 1e-5f;
constexpr float QSCALE = 0.22360679774997896f * 1.4426950408889634f;

constexpr size_t WS_KV = 0;
constexpr size_t WS_Q0 = WS_KV + (size_t)NH * 64 * 3072;
constexpr size_t WS_Q1 = WS_Q0 + (size_t)NH * 64 * 1024;
constexpr size_t WS_O  = WS_Q1 + (size_t)NH * SEQ * 8;
constexpr size_t WS_H  = WS_O + (size_t)SEQ * DM * 2;
constexpr size_t WS_HN = WS_H + (size_t)SEQ * DM * 2;
constexpr size_t WS_U  = WS_HN + (size_t)SEQ * DM * 2;
constexpr size_t WS_ST = WS_U + (size_t)DM * FF * 2;
constexpr size_t WS_ST_BYTES = (size_t)32 * 8 * 64 * 16;
constexpr size_t WS_END = WS_ST + WS_ST_BYTES;

DI rsrc_t mk_rsrc(const void* p, unsigned bytes) { return __builtin_amdgcn_make_buffer_rsrc((void*)p, (short)0, (int)bytes, 0x00020000); }
DI void dma16(unsigned lds, unsigned voff, rsrc_t rs, unsigned soff) {
    asm volatile("s_mov_b32 m0, %0\n\ts_nop 0\n\tbuffer_load_dwordx4 %1, %2, %3 offen lds" ::"s"(lds), "v"(voff), "s"(rs), "s"(soff) : "memory");
}
DI void touch(unsigned lds, unsigned voff, rsrc_t rs) {
    asm volatile("s_mov_b32 m0, %0\n\ts_nop 0\n\tbuffer_load_dword %1, %2, 0 offen lds" ::"s"(lds), "v"(voff), "s"(rs) : "memory");
}
template <int N> DI void wait_bar() { asm volatile("s_waitcnt vmcnt(%0) lgkmcnt(0)\n\ts_barrier" ::"i"(N) : "memory"); }
DI void lds_bar() { asm volatile("s_waitcnt lgkmcnt(0)\n\ts_barrier" ::: "memory"); }
DI unsigned cvtpk(float lo, float hi) {
    typedef __attribute__((ext_vector_type(2))) float f32x2;
    return __builtin_bit_cast(unsigned, __builtin_convertvector(f32x2{lo, hi}, bf16x2v));
}
DI float bf_lo(unsigned u) { return __uint_as_float(u << 16); }
DI float bf_hi(unsigned u) { return __uint_as_float(u & 0xffff0000u); }
DI bf16x4 ldtr(const LAS char* p) { return __builtin_amdgcn_ds_read_tr16_b64_v4i16((LAS bf16x4*)p); }
DI bf16x8 ldtr2(const LAS char* p0, const LAS char* p1) {
    const bf16x4 a = ldtr(p0), b = ldtr(p1);
    return __builtin_shufflevector(a, b, 0, 1, 2, 3, 4, 5, 6, 7);
}
DI bf16x8 ld128(const LAS char* p) { return *(const LAS bf16x8*)p; }
DI f32x16 mfma32(bf16x8 a, bf16x8 b, f32x16 c) { return __builtin_amdgcn_mfma_f32_32x32x16_bf16(a, b, c, 0, 0, 0); }
DI f32x16 mfma32k8(bf16x4 a, bf16x4 b, f32x16 c) { return __builtin_amdgcn_mfma_f32_32x32x8bf16_1k(a, b, c, 0, 0, 0); }
DI f32x4 mfma16(bf16x8 a, bf16x8 b, f32x4 c) { return __builtin_amdgcn_mfma_f32_16x16x32_bf16(a, b, c, 0, 0, 0); }
DI float dot2(unsigned a, unsigned b, float c) {
    return __builtin_amdgcn_fdot2_f32_bf16(__builtin_bit_cast(bf16x2v, a), __builtin_bit_cast(bf16x2v, b), c, false);
}
DI float xhalf_sum(float v) {
    auto r = __builtin_amdgcn_permlane32_swap(__float_as_uint(v), __float_as_uint(v), false, false);
    return __uint_as_float(r[0]) + __uint_as_float(r[1]);
}
DI float xhalf_max(float v) {
    auto r = __builtin_amdgcn_permlane32_swap(__float_as_uint(v), __float_as_uint(v), false, false);
    return __builtin_fmaxf(__uint_as_float(r[0]), __uint_as_float(r[1]));
}
DI int uni(int v) { return __builtin_amdgcn_readfirstlane(v); }
#define PIN(x) asm volatile("" : "+v"(x))
#define SBAR() __builtin_amdgcn_sched_barrier(0)
#define ST_AUX 0
DI void st64(rsrc_t rs, unsigned off, u32x2 v) { __builtin_amdgcn_raw_buffer_store_b64(v, rs, off, 0, ST_AUX); }
DI void st128(rsrc_t rs, unsigned off, u32x4 v) { __builtin_amdgcn_raw_buffer_store_b128(v, rs, off, 0, ST_AUX); }
DI u32x2 ld64g(rsrc_t rs, unsigned off) { return __builtin_bit_cast(u32x2, __builtin_amdgcn_raw_buffer_load_b64(rs, off, 0, 0)); }
DI float rbf(float v) { return bf_lo(cvtpk(v, 0.f)); }
DI void ln_scalars(float s, float ss, float& mean, float& rstd) {
    const float m = s * (1.f / DM), var = fmaxf(ss * (1.f / DM) - m * m, 0.f);
    mean = rbf(m);
    rstd = __builtin_amdgcn_rcpf(rbf(__builtin_amdgcn_sqrtf(rbf(rbf(var) + EPS))));
}
#define PROF_ARG
#define PROF_PASS
#define PROF_DECL
#define MARK(ph, i)
#define PROF_END(ph)

template <int NP, int NT, class Issue, class Work> DI void ring_loader(Issue issue, Work work) {
    static_assert(NT >= 5, "ring depth");
    issue(0); issue(1);
    wait_bar<NP>();
    issue(2); issue(3);
#pragma unroll 1
    for (int t = 0; t + 1 < NT; ++t) {
        work(t);
        if (t + 3 < NT) wait_bar<2 * NP>(); else if (t + 2 < NT) wait_bar<NP>(); else wait_bar<0>();
        if (t + 4 < NT) issue(t + 4);
    }
    work(NT - 1);
}
template <int NP, int NT, class Issue> DI void ring_loader(Issue issue) { ring_loader<NP, NT>(issue, [](int) {}); }

struct Params {
    const uint16_t* x; const uint16_t* wqkv; const uint16_t* wo; const uint16_t* w1; const uint16_t* b1; const uint16_t* w2; const uint16_t* b2;
    uint16_t* out; char* ws;
};

constexpr int A_STAGE = 32768, A_NT = 10, A_STAT = A_STAGE * 4;
constexpr int A_LDS = A_STAT + 2048 + 4 * 256;

template <bool ISV>
DI void phaseA_body(const Params& p, LAS char* sm, int rb, int ct PROF_ARG) {
    const int tid = threadIdx.x, l = tid & 63, w = uni(tid >> 6), r = l & 31, h = l >> 5;
    const int row0 = rb * 128, col0 = ct * 128;
    const unsigned sm0 = (unsigned)(uintptr_t)sm;
    LAS float* sa = (LAS float*)(sm + A_STAT);
    LAS float* sb = sa + 128;
    LAS float* cst = sa + 256;
    MARK(0, 0);
    if (w >= 4) {
        const bool isX = w < 6;
        const rsrc_t rs = mk_rsrc(isX ? (const void*)p.x : (const void*)p.wqkv, isX ? SEQ * DM * 2 : DM * 3 * DM * 2);
        unsigned voff[2];
#pragma unroll
        for (int j = 0; j < 2; ++j) {
            const int u = (w & 1) * 8 + j;
            const int ra = 8 * u + (l >> 3), ca = (l & 7) ^ ((ra >> 1) & 7);
            const int kb = 4 * u + (l >> 4), cb = (l & 15) ^ ((kb & 3) << 2);
            voff[j] = isX ? (unsigned)((row0 + ra) * (DM * 2) + ca * 16) : (unsigned)(kb * (3 * DM * 2) + col0 * 2 + cb * 16);
        }
        const unsigned kstep = isX ? 128u : 64u * (3 * DM * 2), pstep = isX ? 16u * (DM * 2) : 8u * (3 * DM * 2);
        const unsigned ldst = sm0 + (w - 4) * 8192;
        ring_loader<8, A_NT>([&](int t) {
#pragma unroll
            for (int j = 0; j < 8; ++j) dma16(ldst + (t & 3) * A_STAGE + j * 1024, voff[j & 1], rs, kstep * t + pstep * (j >> 1));
        });
        lds_bar();
        return;
    }
    const int wm = w >> 1, wn = w & 1;
    const int tb0 = 64 * wm + 32 * wn, tb1 = tb0 ^ 32, cb0 = 64 * wn + 32 * wm, cb1 = cb0 ^ 32;
    const int g = l >> 4, q = (l & 15) >> 2, p4 = l & 3;
    unsigned xa[4];
#pragma unroll
    for (int ks = 0; ks < 4; ++ks) xa[ks] = ((tb0 + r) * 128 + ((h ^ ((r >> 1) & 7)) << 4)) ^ (ks << 5);
    const unsigned wa = 16384 + (8 * (g >> 1) + q) * 256 + ((((cb0 >> 3) + 2 * (g & 1) + (p4 >> 1)) ^ (q << 2)) << 4) + (p4 & 1) * 8;
    f32x16 acc[2][2] = {};
    struct Frag { bf16x8 x0, x1, w0, w1; } F0, F1;
    auto rd = [&](Frag& F, const LAS char* st, int ks) {
        F.x0 = ld128(st + xa[ks]);
        F.w0 = ldtr2(st + wa + ks * 4096, st + wa + ks * 4096 + 1024);
        F.x1 = ld128(st + (xa[ks] ^ 4096));
        F.w1 = ldtr2(st + (wa ^ 64) + ks * 4096, st + (wa ^ 64) + ks * 4096 + 1024);
        SBAR();
    };
    auto mf = [&](bf16x8 x, bf16x8 wv, f32x16 c) { return ISV ? mfma32(x, wv, c) : mfma32(wv, x, c); };
    float xs0 = 0.f, xs1 = 0.f, xq0 = 0.f, xq1 = 0.f;
    f32x16 csum = {};
    const bf16x8 ones = __builtin_bit_cast(bf16x8, u32x4{0x3f803f80u, 0x3f803f80u, 0x3f803f80u, 0x3f803f80u});
    auto st2 = [&](unsigned u) {
        float lo, hi;
        asm volatile("v_lshlrev_b32 %0, 16, %6\n\tv_and_b32 %1, 0xffff0000, %6\n\tv_add_f32 %2, %2, %0\n\tv_add_f32 %3, %3, %1\n\tv_fmac_f32 %4, %0, %0\n\tv_fmac_f32 %5, %1, %1"
                     : "=&v"(lo), "=&v"(hi), "+v"(xs0), "+v"(xs1), "+v"(xq0), "+v"(xq1) : "v"(u));
        SBAR();
    };
    auto mm = [&](const Frag& F) {
        const u32x4 xu = __builtin_bit_cast(u32x4, F.x0);
        acc[0][0] = mf(F.x0, F.w0, acc[0][0]); SBAR(); st2(xu[0]);
        acc[1][0] = mf(F.x1, F.w0, acc[1][0]); SBAR(); st2(xu[1]);
        csum = mf(ones, F.w0, csum); SBAR(); st2(xu[2]);
        acc[0][1] = mf(F.x0, F.w1, acc[0][1]); SBAR(); st2(xu[3]);
        acc[1][1] = mf(F.x1, F.w1, acc[1][1]); SBAR();
    };
    lds_bar();
    MARK(0, 1);
    rd(F0, sm, 0);
#pragma unroll
    for (int t = 0; t < A_NT; ++t) {
        const LAS char* st = sm + (t & 3) * A_STAGE;
        rd(F1, st, 1); mm(F0);
        rd(F0, st, 2); mm(F1);
        rd(F1, st, 3); mm(F0);
        if (t + 1 < A_NT) { lds_bar(); rd(F0, sm + ((t + 1) & 3) * A_STAGE, 0); }
        mm(F1);
    }
    asm volatile("s_waitcnt vmcnt(0)" ::: "memory");
    PIN(csum);
    MARK(0, 2);
    {
        float mean, ra;
        ln_scalars(xhalf_sum(xs0 + xs1), xhalf_sum(xq0 + xq1), mean, ra);
        if (!ISV && ct < 5) ra *= QSCALE;
        if (h == 0) { sa[tb0 + r] = ra; sb[tb0 + r] = -mean * ra; }
        if (ISV) { if (h == 0) cst[cb0 + r] = csum[0]; }
        else if (r == 0) {
#pragma unroll
            for (int gg = 0; gg < 4; ++gg) *(LAS f32x4*)(cst + cb0 + 8 * gg + 4 * h) = f32x4{csum[4 * gg], csum[4 * gg + 1], csum[4 * gg + 2], csum[4 * gg + 3]};
        }
    }
    lds_bar();
    const rsrc_t rw = mk_rsrc(p.ws, (unsigned)WS_END);
    if (ISV) {
#pragma unroll
        for (int a = 0; a < 2; ++a) {
            const int tb = a ? tb1 : tb0, T = rb * 4 + (tb >> 5);
            f32x4 av[4], bv[4];
#pragma unroll
            for (int gg = 0; gg < 4; ++gg) { av[gg] = *(const LAS f32x4*)(sa + tb + 8 * gg + 4 * h); bv[gg] = *(const LAS f32x4*)(sb + tb + 8 * gg + 4 * h); }
#pragma unroll
            for (int j = 0; j < 2; ++j) {
                const int cl = (j ? cb1 : cb0) + r;
                const float cs = cst[cl];
                const int c = col0 + cl - 2 * DM, head = c / 20, d = c - head * 20;
                const unsigned dst = (unsigned)WS_KV + (head * 64 + T) * 3072 + 1536 + h * 384 + d * 16;
#pragma unroll
                for (int s = 0; s < 2; ++s) {
                    u32x4 v;
#pragma unroll
                    for (int i = 0; i < 4; ++i) {
                        const int e = 8 * s + 2 * i;
                        v[i] = cvtpk(av[e >> 2][e & 3] * acc[a][j][e] + bv[e >> 2][e & 3] * cs, av[e >> 2][(e & 3) + 1] * acc[a][j][e + 1] + bv[e >> 2][(e & 3) + 1] * cs);
                    }
                    st128(rw, dst + s * 768, v);
                    if (d >= 16) {
                        const unsigned c1 = d == 16 ? 0x3f803f80u : 0u;
                        st128(rw, dst + s * 768 + 64, u32x4{c1, c1, c1, c1});
                    }
                }
            }
        }
    } else {
        const bool isQ = ct < 5;
        const float ra0 = sa[tb0 + r], rb0 = sb[tb0 + r], ra1 = sa[tb1 + r], rb1 = sb[tb1 + r];
#pragma unroll
        for (int j = 0; j < 2; ++j)
#pragma unroll
            for (int gg = 0; gg < 4; ++gg) {
                const int cl = (j ? cb1 : cb0) + 8 * gg + 4 * h;
                const f32x4 cs = *(const LAS f32x4*)(cst + cl);
                const int c4 = (col0 + cl - (isQ ? 0 : DM)) >> 2, head = c4 / 5, grp = c4 - head * 5;
                const unsigned in0 = ((grp >> 1) * 32 + r) * 16 + (grp & 1) * 8;
#pragma unroll
                for (int a = 0; a < 2; ++a) {
                    const float ra = a ? ra1 : ra0, rbb = a ? rb1 : rb0;
                    const int T = rb * 4 + ((a ? tb1 : tb0) >> 5);
                    u32x2 v;
                    v[0] = cvtpk(ra * acc[a][j][4 * gg] + rbb * cs[0], ra * acc[a][j][4 * gg + 1] + rbb * cs[1]);
                    v[1] = cvtpk(ra * acc[a][j][4 * gg + 2] + rbb * cs[2], ra * acc[a][j][4 * gg + 3] + rbb * cs[3]);
                    if (isQ) {
                        st64(rw, grp < 4 ? (unsigned)WS_Q0 + (head * 64 + T) * 1024 + in0 : (unsigned)WS_Q1 + (head * SEQ + T * 32 + r) * 8, v);
                    } else {
                        const unsigned base = (unsigned)WS_KV + (head * 64 + T) * 3072;
                        st64(rw, base + (grp < 4 ? in0 : 1024 + r * 8), v);
                        if (grp == 4) st64(rw, base + 1280 + r * 8, u32x2{0x3f803f80u, 0x00003f80u});
                    }
                }
            }
    }
}
DI void phaseA(const Params& p, LAS char* sm, int b PROF_ARG) {
    const int g = b & 7, i = b >> 3, rb = 4 * (g & 3) + (i & 3), ct = (g >> 2) * 8 + (i >> 2);
    {
        const int l = threadIdx.x & 63, w = uni(threadIdx.x >> 6);
        if (w < 4) {
            const unsigned scr = (unsigned)(uintptr_t)sm + A_STAT + 2048 + w * 256;
            touch(scr, b * 10240 + w * 2560 + l * 64, mk_rsrc(p.x, SEQ * DM * 2));
            touch(scr, b * 9600 + w * 2400 + l * 64, mk_rsrc(p.wqkv, DM * 3 * DM * 2));
        }
    }
    if (ct >= 15) { asm volatile("s_waitcnt vmcnt(0)" ::: "memory"); return; }
    if (ct >= 10) phaseA_body<true>(p, sm, rb, ct PROF_PASS); else phaseA_body<false>(p, sm, rb, ct PROF_PASS);
    MARK(0, 3);
}

constexpr int B_STAGE = 8 * 3072, B_NS = 3, B_NT = 8, B_FLAG = B_STAGE * B_NS;
constexpr int B_LDS = B_FLAG + 64 + 8 * 256;

DI u32x2 neg_split3(float m) {
    const unsigned hi = cvtpk(m, 0.f) & 0xffffu; const float r1 = m - bf_lo(hi);
    const unsigned mid = cvtpk(r1, 0.f) & 0xffffu; const float r2 = r1 - bf_lo(mid);
    const unsigned lo = cvtpk(r2, 0.f) & 0xffffu;
    return u32x2{(hi | (mid << 16)) ^ 0x80008000u, lo ^ 0x8000u};
}
DI float max16(const f32x16& s) {
    float a = __builtin_fmaxf(__builtin_fmaxf(s[0], s[1]), s[2]), b = __builtin_fmaxf(__builtin_fmaxf(s[3], s[4]), s[5]);
    a = __builtin_fmaxf(__builtin_fmaxf(a, s[6]), s[7]); b = __builtin_fmaxf(__builtin_fmaxf(b, s[8]), s[9]);
    a = __builtin_fmaxf(__builtin_fmaxf(a, s[10]), s[11]); b = __builtin_fmaxf(__builtin_fmaxf(b, s[12]), s[13]);
    a = __builtin_fmaxf(__builtin_fmaxf(a, s[14]), s[15]);
    return __builtin_fmaxf(a, b);
}
DI bf16x4 ld64(const LAS char* p) { return *(const LAS bf16x4*)p; }

DI void phaseB(const Params& p, LAS char* sm, int b PROF_ARG) {
    const int tid = threadIdx.x, l = tid & 63, w = uni(tid >> 6), r = l & 31, h = l >> 5;
    const int g = b & 7, i = b >> 3, head = 4 * g + (i >> 3);
    const int kh = w >> 2, TA = (i & 7) * 8 + (w & 3) * 2;
    const unsigned sm0 = (unsigned)(uintptr_t)sm;
    const rsrc_t rs = mk_rsrc(p.ws + WS_KV + (size_t)head * 64 * 3072, 64 * 3072);
    const unsigned voff = (kh * 32 + (w & 3)) * 3072 + l * 16;
    auto issue = [&](int t, int slot) {
#pragma unroll
        for (int j = 0; j < 3; ++j) dma16(sm0 + slot * B_STAGE + w * 3072 + j * 1024, voff, rs, t * 4 * 3072 + j * 1024);
    };
    MARK(1, 0);
    issue(0, 0); issue(1, 1);
    const char* qp0 = p.ws + WS_Q0 + ((size_t)(head * 64 + TA) * 64 + l) * 16;
    const char* qp1 = p.ws + WS_Q1 + ((size_t)head * SEQ + TA * 32 + r) * 8;
    bf16x8 qA0 = *(const bf16x8*)qp0, qB0 = *(const bf16x8*)(qp0 + 1024);
    u32x2 qA1u = *(const u32x2*)qp1, qB1u = *(const u32x2*)(qp1 + 256);
    PIN(qA0); PIN(qB0); PIN(qA1u); PIN(qB1u);
    if (h) { qA1u = u32x2{0u, 0u}; qB1u = u32x2{0u, 0u}; }
    const unsigned k0o = l * 16, k1o = 1024 + l * 8, vo = 1536 + h * 384 + (r < 22 ? r : 22) * 16;
    const LAS char* sw = sm + kh * 4 * 3072;
    f32x16 oA, oB;
    float lsum, mA, mB;
    auto scores = [&](const LAS char* tp, bf16x8 q0, u32x2 q1) {
        return mfma32k8(ld64(tp + k1o), __builtin_bit_cast(bf16x4, q1), mfma32(ld128(tp + k0o), q0, f32x16{}));
    };
    for (int pass = 0;; ++pass) {
        if (pass == 1) {
            if (h) { qA1u = u32x2{0u, 0u}; qB1u = u32x2{0u, 0u}; }
            mA = -INFINITY; mB = -INFINITY;
            issue(0, 0); issue(1, 1);
            for (int t = 0; t < B_NT; ++t) {
                if (t + 1 < B_NT) wait_bar<3>(); else wait_bar<0>();
                if (t + 2 < B_NT) issue(t + 2, (t + 2) % B_NS);
                const LAS char* st = sm + (t % B_NS) * B_STAGE;
#pragma unroll 1
                for (int kt = 0; kt < 8; ++kt) {
                    mA = __builtin_fmaxf(mA, max16(scores(st + kt * 3072, qA0, qA1u)));
                    mB = __builtin_fmaxf(mB, max16(scores(st + kt * 3072, qB0, qB1u)));
                }
            }
            mA = xhalf_max(mA); mB = xhalf_max(mB);
            lds_bar();
            if (h) { qA1u = neg_split3(mA); qB1u = neg_split3(mB); }
            issue(0, 0); issue(1, 1);
        }
        wait_bar<3>();
        if (pass == 0) {
            const f32x16 s0 = scores(sw, qA0, qA1u), s1 = scores(sw, qB0, qB1u);
            mA = xhalf_max(max16(s0)); mB = xhalf_max(max16(s1));
            if (h) { qA1u = neg_split3(mA); qB1u = neg_split3(mB); }
        }
        MARK(1, 1);
        const bf16x4 qA1 = __builtin_bit_cast(bf16x4, qA1u), qB1 = __builtin_bit_cast(bf16x4, qB1u);
        oA = f32x16{}; oB = f32x16{};
        bf16x8 kf0 = ld128(sw + k0o), vfa = ld128(sw + vo), vfb, vf1 = bf16x8{};
        bf16x4 kf1 = ld64(sw + k1o);
        f32x16 sA = mfma32k8(kf1, qA1, mfma32(kf0, qA0, f32x16{})), sB;
        u32x4 p0, p1 = u32x4{0u, 0u, 0u, 0u};
        int slot = 0;
#define EX(v) __builtin_amdgcn_exp2f(v)
#define BF(P) __builtin_bit_cast(bf16x8, P)
#define GRP(S, a, P, c) do { \
            const float e0_ = EX(S[a]), e1_ = EX(S[a + 1]), e2_ = EX(S[a + 2]), e3_ = EX(S[a + 3]); \
            P[c] = cvtpk(e0_, e1_); P[c + 1] = cvtpk(e2_, e3_); PIN(P); SBAR(); } while (0)
#define MF(stmt) do { stmt; SBAR(); } while (0)
#define TILE(TC, TN, V0, V0N) do { \
            MF(sB = mfma32(kf0, qB0, f32x16{}));          GRP(sA, 0, p0, 0); \
            MF(oB = mfma32(vf1, BF(p1), oB)); \
            MF((kf0 = ld128((TN) + k0o), vf1 = ld128((TC) + vo + 768), V0N = ld128((TN) + vo))); \
                                                          GRP(sA, 4, p0, 2); \
            MF(sB = mfma32k8(kf1, qB1, sB)); \
            MF(kf1 = ld64((TN) + k1o));                   GRP(sA, 8, p1, 0); \
            MF(oA = mfma32(V0, BF(p0), oA));              GRP(sA, 12, p1, 2); \
            asm volatile("" : "+v"(kf0), "+v"(kf1), "+v"(vf1), "+v"(V0N)); SBAR(); \
            MF(sA = mfma32(kf0, qA0, f32x16{}));          GRP(sB, 0, p0, 0); \
            MF(oA = mfma32(vf1, BF(p1), oA));             GRP(sB, 4, p0, 2); \
            MF(sA = mfma32k8(kf1, qA1, sA));              GRP(sB, 8, p1, 0); \
            MF(oB = mfma32(V0, BF(p0), oB));              GRP(sB, 12, p1, 2); } while (0)
#pragma unroll 1
        for (int t = 0; t < B_NT; ++t) {
            const int nslot = slot == 2 ? 0 : slot + 1;
            const LAS char* st = sw + slot * B_STAGE;
            const LAS char* sn = sw + nslot * B_STAGE;
            TILE(st, st + 1 * 3072, vfa, vfb);
            TILE(st + 1 * 3072, st + 2 * 3072, vfb, vfa);
            TILE(st + 2 * 3072, st + 3 * 3072, vfa, vfb);
            if (t == 1 && pass == 0) wait_bar<2>(); else
            wait_bar<0>();
            if (t + 2 < B_NT) issue(t + 2, nslot == 2 ? 0 : nslot + 1);
            if (t == 0 && pass == 0) {
                const unsigned scr = sm0 + B_FLAG + 64 + w * 256;
                if (w < 6) {
                    touch(scr, b * 24576 + w * 4096 + l * 64, mk_rsrc(p.w1, SEQ * FF * 2));
                    touch(scr, b * 24576 + w * 4096 + l * 64, mk_rsrc(p.w2, SEQ * FF * 2));
                } else {
                    touch(scr, b * 3200 + l * 64, mk_rsrc(p.wo, w == 6 ? DM * DM * 2 : 0));
                    touch(scr, l * 64, mk_rsrc(w == 6 ? p.b1 : p.b2, w == 6 ? FF * 2 : SEQ * 2));
                }
            }
            TILE(st + 3 * 3072, sn, vfb, vfa);
            slot = nslot;
        }
        oB = mfma32(vf1, BF(p1), oB);
#undef TILE
#undef MF
#undef GRP
#undef BF
#undef EX
        MARK(1, 2);
        lds_bar();
        LAS f32x4* red = (LAS f32x4*)sm + (w & 3) * 7 * 64 + l;
        LAS float* mex = (LAS float*)((LAS f32x4*)sm + ((w & 3) * 7 + 6) * 64);
        f32x16 o = kh ? oB : oA;
        {
            const f32x16 give = kh ? oA : oB;
#pragma unroll
            for (int e = 0; e < 3; ++e) red[(kh * 3 + e) * 64] = f32x4{give[4 * e], give[4 * e + 1], give[4 * e + 2], give[4 * e + 3]};
            mex[kh * 64 + l] = kh ? mA : mB;
        }
        lds_bar();
        {
            const float mo = mex[(kh ^ 1) * 64 + l], mm = kh ? mB : mA, mx = __builtin_fmaxf(mo, mm);
            const float fo = __builtin_amdgcn_exp2f(mo - mx), fm = __builtin_amdgcn_exp2f(mm - mx);
#pragma unroll
            for (int e = 0; e < 3; ++e) {
                const f32x4 v = red[((kh ^ 1) * 3 + e) * 64];
#pragma unroll
                for (int k = 0; k < 4; ++k) o[4 * e + k] = o[4 * e + k] * fm + v[k] * fo;
            }
        }
        oA = o;
        {
            auto rr = __builtin_amdgcn_permlane32_swap(__float_as_uint(o[8]), __float_as_uint(o[8]), false, false);
            lsum = __uint_as_float(rr[1]);
        }
        if (pass == 1) break;
        bool ok = lsum > 0x1p-30f && lsum < 0x1p64f;
#pragma unroll
        for (int e = 0; e < 12; ++e) ok = ok && (e >= 8 && h ? true : __builtin_fabsf(o[e]) < INFINITY);
        LAS int* flag = (LAS int*)(sm + B_FLAG);
        const bool wave_ok = __all(ok);
        if (l == 0) flag[w] = wave_ok ? 0 : 1;
        lds_bar();
        const u32x4 f0 = *(LAS u32x4*)flag, f1 = *(LAS u32x4*)(flag + 4);
        if (!uni(f0[0] | f0[1] | f0[2] | f0[3] | f1[0] | f1[1] | f1[2] | f1[3])) break;
        lds_bar();
    }
    const float inv = __builtin_amdgcn_rcpf(lsum);
    const rsrc_t rw = mk_rsrc(p.ws, (unsigned)WS_END);
    const unsigned orow = (unsigned)WS_O + ((TA + kh) * 32 + r) * (DM * 2);
#pragma unroll
    for (int e = 0; e < 3; ++e) {
        if (e < 2 || h == 0) {
            const int gam = head * 5 + 2 * e + h;
            const int G = gam & 7, pos = (gam & ~7) + 2 * (G & 3) + (G >> 2);
            u32x2 v;
            v[0] = cvtpk(oA[4 * e] * inv, oA[4 * e + 1] * inv); v[1] = cvtpk(oA[4 * e + 2] * inv, oA[4 * e + 3] * inv);
            st64(rw, orow + pos * 8, v);
        }
    }
    MARK(1, 3);
}

constexpr int C_STAGE = 36864, C_NT = 5, C_RED = C_STAGE * 4;
constexpr int C_LDS = C_RED + 8 * 64 * 8 + 64;

DI void phaseC(const Params& p, LAS char* sm, int b PROF_ARG) {
    const int tid = threadIdx.x, l = tid & 63, w = uni(tid >> 6);
    const int gx = b & 7, ix = b >> 3, rb = 4 * gx + (ix >> 3), cb = ix & 7;
    const int row0 = rb * 64, col0 = cb * 80;
    const unsigned sm0 = (unsigned)(uintptr_t)sm;
    const int tk = l & 15, g = l >> 4, q = (l & 15) >> 2, p4 = l & 3;
    const int row = row0 + 16 * (w & 3) + tk;
    const rsrc_t rw = mk_rsrc(p.ws, (unsigned)WS_END);
    float hv[20];
    unsigned long long* gran = (unsigned long long*)(p.ws + WS_ST) + (size_t)rb * 8 * 64 * 2;
    MARK(2, 0);
    if (w >= 4) {
        const int lw = w - 4;
        const rsrc_t rsO = mk_rsrc(p.ws + WS_O, SEQ * DM * 2), rsW = mk_rsrc(p.wo, DM * DM * 2);
        unsigned vo[4], vw[5];
#pragma unroll
        for (int j = 0; j < 4; ++j) { const int rr = 4 * (lw * 4 + j) + (l >> 4), c = (l & 15) ^ (rr & 15); vo[j] = (row0 + rr) * (DM * 2) + c * 16; }
#pragma unroll
        for (int j = 0; j < 5; ++j) { const int ch = (lw * 5 + j) * 64 + l, kr = ch / 10, c = ch - kr * 10; vw[j] = kr * (DM * 2) + col0 * 2 + c * 16; }
        ring_loader<9, C_NT>([&](int t) {
            const unsigned base = sm0 + (t & 3) * C_STAGE;
#pragma unroll
            for (int j = 0; j < 4; ++j) dma16(base + (lw * 4 + j) * 1024, vo[j], rsO, t * 256);
#pragma unroll
            for (int j = 0; j < 5; ++j) dma16(base + 16384 + (lw * 5 + j) * 1024, vw[j], rsW, t * 128 * (DM * 2));
        });
    } else {
        u32x2 xv[5];
        {
            const rsrc_t rx = mk_rsrc(p.x, SEQ * DM * 2);
#pragma unroll
            for (int j = 0; j < 5; ++j) xv[j] = ld64g(rx, (row * DM + col0 + 16 * j + 4 * g) * 2);
        }
        unsigned oa[4];
#pragma unroll
        for (int ks = 0; ks < 4; ++ks) oa[ks] = (16 * w + tk) * 256 + (((4 * ks + g) ^ tk) << 4);
        const unsigned wa = 16384 + (4 * g + q) * 160 + p4 * 8;
        f32x4 acc[5] = {};
        struct Frag { bf16x8 o, w[5]; } F0, F1;
        auto rd = [&](Frag& F, const LAS char* st, int ks) {
            F.o = ld128(st + oa[ks]);
#pragma unroll
            for (int j = 0; j < 5; ++j) F.w[j] = ldtr2(st + wa + ks * 32 * 160 + j * 32, st + wa + ks * 32 * 160 + j * 32 + 16 * 160);
            SBAR();
        };
        auto mm = [&](const Frag& F) {
#pragma unroll
            for (int j = 0; j < 5; ++j) acc[j] = mfma16(F.w[j], F.o, acc[j]);
            SBAR();
        };
        lds_bar();
        MARK(2, 1);
        rd(F0, sm, 0);
#pragma unroll
        for (int t = 0; t < C_NT; ++t) {
            const LAS char* st = sm + (t & 3) * C_STAGE;
            rd(F1, st, 1); mm(F0);
            rd(F0, st, 2); mm(F1);
            rd(F1, st, 3); mm(F0);
            if (t + 1 < C_NT) { lds_bar(); rd(F0, sm + ((t + 1) & 3) * C_STAGE, 0); }
            mm(F1);
        }
        MARK(2, 2);
        float s = 0.f, ss = 0.f;
        u32x2 hb[5];
#pragma unroll
        for (int j = 0; j < 5; ++j) {
            const f32x4 a = acc[j];
            hb[j][0] = cvtpk(a[0] + bf_lo(xv[j][0]), a[1] + bf_hi(xv[j][0])); hb[j][1] = cvtpk(a[2] + bf_lo(xv[j][1]), a[3] + bf_hi(xv[j][1]));
            hv[4 * j] = bf_lo(hb[j][0]); hv[4 * j + 1] = bf_hi(hb[j][0]); hv[4 * j + 2] = bf_lo(hb[j][1]); hv[4 * j + 3] = bf_hi(hb[j][1]);
#pragma unroll
            for (int e = 0; e < 4; ++e) { s += hv[4 * j + e]; ss += hv[4 * j + e] * hv[4 * j + e]; }
        }
        s += __shfl_xor(s, 16); ss += __shfl_xor(ss, 16); s = xhalf_sum(s); ss = xhalf_sum(ss);
        if (g < 2) {
            const unsigned long long v = (1ull << 32) | __float_as_uint(g == 0 ? s : ss);
            __hip_atomic_store(gran + ((size_t)cb * 64 + 16 * w + tk) * 2 + g, v, __ATOMIC_RELAXED, __HIP_MEMORY_SCOPE_AGENT);
        }
#pragma unroll
        for (int j = 0; j < 5; ++j) st64(rw, (unsigned)WS_H + (row * DM + col0 + 16 * j + 4 * g) * 2, hb[j]);
    }
    MARK(2, 3);
    LAS float* fin = (LAS float*)(sm + C_RED);
    {
        unsigned long long a, c; unsigned spins = 0;
        for (;;) {
            a = __hip_atomic_load(gran + ((size_t)w * 64 + l) * 2, __ATOMIC_RELAXED, __HIP_MEMORY_SCOPE_AGENT);
            c = __hip_atomic_load(gran + ((size_t)w * 64 + l) * 2 + 1, __ATOMIC_RELAXED, __HIP_MEMORY_SCOPE_AGENT);
            if (__all((a >> 32) == 1ull && (c >> 32) == 1ull)) break;
            if (++spins > (1u << 20)) break;
            __builtin_amdgcn_s_sleep(1);
        }
        *(LAS u32x2*)(fin + (w * 64 + l) * 2) = u32x2{(unsigned)a, (unsigned)c};
    }
    lds_bar();
    MARK(2, 4);
    if (w < 4) {
        float s = 0.f, ss = 0.f;
#pragma unroll
        for (int j = 0; j < 8; ++j) { const u32x2 v = *(const LAS u32x2*)(fin + (j * 64 + 16 * w + tk) * 2); s += __uint_as_float(v[0]); ss += __uint_as_float(v[1]); }
        const float m = s * (1.f / DM), var = fmaxf(ss * (1.f / DM) - m * m, 0.f);
        const float r1 = __builtin_amdgcn_rsqf(var + EPS);
        const float r2 = __builtin_amdgcn_rsqf(var * r1 * r1 + EPS), rr = r1 * r2;
#pragma unroll
        for (int j = 0; j < 5; ++j) {
            u32x2 v;
            v[0] = cvtpk((hv[4 * j] - m) * rr, (hv[4 * j + 1] - m) * rr); v[1] = cvtpk((hv[4 * j + 2] - m) * rr, (hv[4 * j + 3] - m) * rr);
            st64(rw, (unsigned)WS_HN + (row * DM + col0 + 16 * j + 4 * g) * 2, v);
        }
    }
    MARK(2, 5);
}

constexpr int D_STAGE = 32768, D_NT = 16;
constexpr int D_LDS = D_STAGE * 4;

DI float gelu_tanh(float v) {
    const float y = v * (0.7978845608028654f + 0.7978845608028654f * 0.044715f * v * v);
    return v * __builtin_amdgcn_rcpf(1.f + __builtin_amdgcn_exp2f(-2.f * 1.4426950408889634f * y));
}
DI void phaseD(const Params& p, LAS char* sm, int b PROF_ARG) {
    const int gx = b & 7, ix = b >> 3;
    if (ix >= 30) return;
    const int db = 5 * (gx & 1) + ix % 5, fb = 6 * (gx >> 1) + ix / 5;
    const int tid = threadIdx.x, l = tid & 63, w = uni(tid >> 6);
    const unsigned sm0 = (unsigned)(uintptr_t)sm;
    MARK(3, 0);
    if (w >= 4) {
        const bool isH = w < 6;
        const int pitch = isH ? DM * 2 : FF * 2;
        const rsrc_t rs = mk_rsrc(isH ? (const void*)(p.ws + WS_HN) : (const void*)p.w1, isH ? SEQ * DM * 2 : SEQ * FF * 2);
        const int kr = 64 * (w & 1) + (l >> 3);
        const unsigned voff = kr * pitch + (isH ? db : fb) * 128 + (((l & 7) ^ (((kr >> 1) & 1) << 2)) << 4);
        const unsigned ldst = sm0 + (w - 4) * 8192;
        ring_loader<8, D_NT>([&](int t) {
#pragma unroll
            for (int j = 0; j < 8; ++j) dma16(ldst + (t & 3) * D_STAGE + j * 1024, voff, rs, (unsigned)((t * 128 + j * 8) * pitch));
        });
        return;
    }
    const int wf = w >> 1, wd = w & 1, r = l & 31, h = l >> 5;
    const int g = l >> 4, q = (l & 15) >> 2, p4 = l & 3;
    u32x2 bv[4];
    {
        const rsrc_t rb1 = mk_rsrc(p.b1, FF * 2);
#pragma unroll
        for (int e = 0; e < 4; ++e) bv[e] = ld64g(rb1, (fb * 64 + 32 * wf + 8 * e + 4 * h) * 2);
    }
    const unsigned rowo = (8 * (g >> 1) + q) * 128 + (p4 & 1) * 8, sw = ((q >> 1) & 1) << 2;
    const unsigned ha = rowo + (((4 * wd + 2 * (g & 1) + (p4 >> 1)) ^ sw) << 4);
    const unsigned wa = 16384 + rowo + (((4 * wf + 2 * (g & 1) + (p4 >> 1)) ^ sw) << 4);
    f32x16 acc = {};
    struct Frag { bf16x8 h, w; } F0, F1;
    auto rd = [&](Frag& F, const LAS char* st, int ks) {
        F.h = ldtr2(st + ha + ks * 2048, st + ha + ks * 2048 + 512);
        F.w = ldtr2(st + wa + ks * 2048, st + wa + ks * 2048 + 512);
        SBAR();
    };
    auto mm = [&](const Frag& F) { acc = mfma32(F.w, F.h, acc); SBAR(); };
    lds_bar();
    MARK(3, 1);
    rd(F0, sm, 0);
#pragma unroll
    for (int t = 0; t < D_NT; ++t) {
        const LAS char* st = sm + (t & 3) * D_STAGE;
        rd(F1, st, 1); mm(F0);
        rd(F0, st, 2); mm(F1);
        rd(F1, st, 3); mm(F0);
        rd(F0, st, 4); mm(F1);
        rd(F1, st, 5); mm(F0);
        rd(F0, st, 6); mm(F1);
        rd(F1, st, 7); mm(F0);
        if (t + 1 < D_NT) { lds_bar(); rd(F0, sm + ((t + 1) & 3) * D_STAGE, 0); }
        mm(F1);
    }
    MARK(3, 2);
    {
        const rsrc_t rw = mk_rsrc(p.ws, (unsigned)WS_END);
        const int d = db * 64 + 32 * wd + r;
#pragma unroll
        for (int e = 0; e < 4; ++e) {
            const int f0 = fb * 64 + 32 * wf + 8 * e + 4 * h;
            u32x2 v;
            v[0] = cvtpk(gelu_tanh(acc[4 * e] + bf_lo(bv[e][0])), gelu_tanh(acc[4 * e + 1] + bf_hi(bv[e][0])));
            v[1] = cvtpk(gelu_tanh(acc[4 * e + 2] + bf_lo(bv[e][1])), gelu_tanh(acc[4 * e + 3] + bf_hi(bv[e][1])));
            st64(rw, (unsigned)WS_U + (d * FF + f0) * 2, v);
        }
    }
    MARK(3, 3);
}

constexpr int E_STAGE = 36864, E_NT = 12;
constexpr int E_LDS = E_STAGE * 4;

DI void phaseE(const Params& p, LAS char* sm, int b PROF_ARG) {
    const int gx = b & 7, ix = b >> 3, db = 4 * (gx & 1) + (ix & 3), sb = 8 * (gx >> 1) + (ix >> 2);
    const int d0 = db * 80, s0 = sb * 64;
    const int tid = threadIdx.x, l = tid & 63, w = uni(tid >> 6);
    const unsigned sm0 = (unsigned)(uintptr_t)sm;
    MARK(4, 0);
    if (w >= 4) {
        const int lw = w - 4;
        const rsrc_t rsU = mk_rsrc(p.ws + WS_U, DM * FF * 2), rsW = mk_rsrc(p.w2, FF * SEQ * 2);
        unsigned vu[5], vw[2];
#pragma unroll
        for (int j = 0; j < 5; ++j) { const int rr = 4 * (lw * 5 + j) + (l >> 4), c = (l & 15) ^ (rr & 15); vu[j] = (d0 + rr) * (FF * 2) + c * 16; }
#pragma unroll
        for (int j = 0; j < 2; ++j) {
            const int kr = 8 * (lw * 4 + j) + (l >> 3), fr = ((kr >> 1) & 1) | (((kr >> 3) & 1) << 1);
            vw[j] = kr * (SEQ * 2) + s0 * 2 + (((l & 7) ^ (fr << 1)) << 4);
        }
        ring_loader<9, E_NT>([&](int t) {
            const unsigned base = sm0 + (t & 3) * E_STAGE;
#pragma unroll
            for (int j = 0; j < 5; ++j) dma16(base + (lw * 5 + j) * 1024, vu[j], rsU, t * 256);
#pragma unroll
            for (int j = 0; j < 4; ++j) dma16(base + 20480 + (lw * 4 + j) * 1024, vw[j & 1], rsW, (unsigned)((t * 128 + (j >> 1) * 16) * (SEQ * 2)));
        });
        return;
    }
    const int tk = l & 15, g = l >> 4, q = (l & 15) >> 2, p4 = l & 3;
    const int srow = s0 + 16 * w + tk;
    u32x2 hv[5];
    unsigned b2u;
    {
        const rsrc_t rh = mk_rsrc(p.ws + WS_H, SEQ * DM * 2), rb2 = mk_rsrc(p.b2, SEQ * 2);
#pragma unroll
        for (int j = 0; j < 5; ++j) hv[j] = ld64g(rh, (srow * DM + d0 + 16 * j + 4 * g) * 2);
        b2u = __builtin_amdgcn_raw_buffer_load_b16(rb2, srow * 2, 0, 0);
    }
    const int fr = ((q >> 1) & 1) | ((g & 1) << 1);
    const unsigned wa = 20480 + (8 * g + q) * 128 + (((2 * w + (p4 >> 1)) ^ (fr << 1)) << 4) + (p4 & 1) * 8;
    unsigned uo[4];
#pragma unroll
    for (int ks = 0; ks < 4; ++ks) uo[ks] = tk * 256 + (((4 * ks + g) ^ tk) << 4);
    f32x4 acc[5] = {};
    struct Frag { bf16x8 w, u[5]; } F0, F1;
    auto rd = [&](Frag& F, const LAS char* st, int ks) {
        F.w = ldtr2(st + wa + ks * 4096, st + wa + ks * 4096 + 512);
#pragma unroll
        for (int j = 0; j < 5; ++j) F.u[j] = ld128(st + uo[ks] + j * 4096);
        SBAR();
    };
    auto mm = [&](const Frag& F) {
#pragma unroll
        for (int j = 0; j < 5; ++j) acc[j] = mfma16(F.u[j], F.w, acc[j]);
        SBAR();
    };
    lds_bar();
    MARK(4, 1);
    rd(F0, sm, 0);
#pragma unroll
    for (int t = 0; t < E_NT; ++t) {
        const LAS char* st = sm + (t & 3) * E_STAGE;
        rd(F1, st, 1); mm(F0);
        rd(F0, st, 2); mm(F1);
        rd(F1, st, 3); mm(F0);
        if (t + 1 < E_NT) { lds_bar(); rd(F0, sm + ((t + 1) & 3) * E_STAGE, 0); }
        mm(F1);
    }
    MARK(4, 2);
    {
        const rsrc_t ro = mk_rsrc(p.out, SEQ * DM * 2);
        const float bias = bf_lo(b2u);
#pragma unroll
        for (int j = 0; j < 5; ++j) {
            const f32x4 a = acc[j];
            u32x2 v;
            v[0] = cvtpk(bf_lo(hv[j][0]) + (a[0] + bias), bf_hi(hv[j][0]) + (a[1] + bias));
            v[1] = cvtpk(bf_lo(hv[j][1]) + (a[2] + bias), bf_hi(hv[j][1]) + (a[3] + bias));
            __builtin_amdgcn_raw_buffer_store_b64(v, ro, (srow * DM + d0 + 16 * j + 4 * g) * 2, 0, 0);
        }
    }
    MARK(4, 3);
}

__global__ void __launch_bounds__(512, 2) k_phaseA(Params p) {
    extern __shared__ __attribute__((aligned(16))) char smem[];
    { const int i = blockIdx.x * 512 + threadIdx.x; if (i < (int)(WS_ST_BYTES / 16)) *(u32x4*)(p.ws + WS_ST + (size_t)i * 16) = u32x4{0u, 0u, 0u, 0u}; }
    PROF_DECL phaseA(p, (LAS char*)smem, blockIdx.x PROF_PASS); PROF_END(0);
}
__global__ void __launch_bounds__(512, 2) k_phaseB(Params p) { extern __shared__ __attribute__((aligned(16))) char smem[]; PROF_DECL phaseB(p, (LAS char*)smem, blockIdx.x PROF_PASS); PROF_END(1); }
__global__ void __launch_bounds__(512, 2) k_phaseC(Params p) { extern __shared__ __attribute__((aligned(16))) char smem[]; PROF_DECL phaseC(p, (LAS char*)smem, blockIdx.x PROF_PASS); PROF_END(2); }
__global__ void __launch_bounds__(512, 2) k_phaseD(Params p) { extern __shared__ __attribute__((aligned(16))) char smem[]; PROF_DECL phaseD(p, (LAS char*)smem, blockIdx.x PROF_PASS); PROF_END(3); }
__global__ void __launch_bounds__(512, 2) k_phaseE(Params p) { extern __shared__ __attribute__((aligned(16))) char smem[]; PROF_DECL phaseE(p, (LAS char*)smem, blockIdx.x PROF_PASS); PROF_END(4); }

extern "C" void kernel_launch(void* const* d_in, const int* in_sizes, int n_in, void* d_out, int out_size, void* d_ws, size_t ws_size,
                              hipStream_t stream) {
    Params p;
    p.x = (const uint16_t*)d_in[0]; p.wqkv = (const uint16_t*)d_in[1]; p.wo = (const uint16_t*)d_in[2];
    p.w1 = (const uint16_t*)d_in[4]; p.b1 = (const uint16_t*)d_in[5]; p.w2 = (const uint16_t*)d_in[6]; p.b2 = (const uint16_t*)d_in[7];
    p.out = (uint16_t*)d_out; p.ws = (char*)d_ws;
    (void)hipFuncSetAttribute((const void*)k_phaseA, hipFuncAttributeMaxDynamicSharedMemorySize, A_LDS);
    (void)hipFuncSetAttribute((const void*)k_phaseB, hipFuncAttributeMaxDynamicSharedMemorySize, B_LDS);
    (void)hipFuncSetAttribute((const void*)k_phaseC, hipFuncAttributeMaxDynamicSharedMemorySize, C_LDS);
    (void)hipFuncSetAttribute((const void*)k_phaseD, hipFuncAttributeMaxDynamicSharedMemorySize, D_LDS);
    (void)hipFuncSetAttribute((const void*)k_phaseE, hipFuncAttributeMaxDynamicSharedMemorySize, E_LDS);
    k_phaseA<<<256, 512, A_LDS, stream>>>(p);
    k_phaseB<<<256, 512, B_LDS, stream>>>(p);
    k_phaseC<<<256, 512, C_LDS, stream>>>(p);
    k_phaseD<<<256, 512, D_LDS, stream>>>(p);
    k_phaseE<<<256, 512, E_LDS, stream>>>(p);
}
```

```cpp
#include <hip/hip_runtime.h>
#include <hip/hip_bf16.h>
#include <stdint.h>

#define LAS __attribute__((address_space(3)))
#define DI __device__ __forceinline__
typedef __attribute__((ext_vector_type(8))) short bf16x8;
typedef __attribute__((ext_vector_type(4))) short bf16x4;
typedef __attribute__((ext_vector_type(2))) __bf16 bf16x2v;
typedef __attribute__((ext_vector_type(16))) float f32x16;
typedef __attribute__((ext_vector_type(4))) float f32x4;
typedef __attribute__((ext_vector_type(4))) unsigned u32x4;
typedef __attribute__((ext_vector_type(2))) unsigned u32x2;
typedef __amdgpu_buffer_rsrc_t rsrc_t;

constexpr int SEQ = 2048, DM = 640, NH = 32, FF = 1536;
constexpr float EPS = 1e-5f;
constexpr float QSCALE = 0.22360679774997896f * 1.4426950408889634f;

constexpr size_t WS_KV = 0;
constexpr size_t WS_Q0 = WS_KV + (size_t)NH * 64 * 3072;
constexpr size_t WS_Q1 = WS_Q0 + (size_t)NH * 64 * 1024;
constexpr size_t WS_O  = WS_Q1 + (size_t)NH * SEQ * 8;
constexpr size_t WS_H  = WS_O + (size_t)SEQ * DM * 2;
constexpr size_t WS_HN = WS_H + (size_t)SEQ * DM * 2;
constexpr size_t WS_U  = WS_HN + (size_t)SEQ * DM * 2;
constexpr size_t WS_ST = WS_U + (size_t)DM * FF * 2;
constexpr size_t WS_ST_BYTES = (size_t)32 * 8 * 64 * 16;
constexpr size_t WS_END = WS_ST + WS_ST_BYTES;

DI rsrc_t mk_rsrc(const void* p, unsigned bytes) { return __builtin_amdgcn_make_buffer_rsrc((void*)p, (short)0, (int)bytes, 0x00020000); }
DI void dma16(unsigned lds, unsigned voff, rsrc_t rs, unsigned soff) {
    asm volatile("s_mov_b32 m0, %0\n\ts_nop 0\n\tbuffer_load_dwordx4 %1, %2, %3 offen lds" ::"s"(lds), "v"(voff), "s"(rs), "s"(soff) : "memory");
}
DI void touch(unsigned lds, unsigned voff, rsrc_t rs) {
    asm volatile("s_mov_b32 m0, %0\n\ts_nop 0\n\tbuffer_load_dword %1, %2, 0 offen lds" ::"s"(lds), "v"(voff), "s"(rs) : "memory");
}
template <int N> DI void wait_bar() { asm volatile("s_waitcnt vmcnt(%0) lgkmcnt(0)\n\ts_barrier" ::"i"(N) : "memory"); }
DI void lds_bar() { asm volatile("s_waitcnt lgkmcnt(0)\n\ts_barrier" ::: "memory"); }
DI unsigned cvtpk(float lo, float hi) {
    typedef __attribute__((ext_vector_type(2))) float f32x2;
    return __builtin_bit_cast(unsigned, __builtin_convertvector(f32x2{lo, hi}, bf16x2v));
}
DI float bf_lo(unsigned u) { return __uint_as_float(u << 16); }
DI float bf_hi(unsigned u) { return __uint_as_float(u & 0xffff0000u); }
DI bf16x4 ldtr(const LAS char* p) { return __builtin_amdgcn_ds_read_tr16_b64_v4i16((LAS bf16x4*)p); }
DI bf16x8 ldtr2(const LAS char* p0, const LAS char* p1) {
    const bf16x4 a = ldtr(p0), b = ldtr(p1);
    return __builtin_shufflevector(a, b, 0, 1, 2, 3, 4, 5, 6, 7);
}
DI bf16x8 ld128(const LAS char* p) { return *(const LAS bf16x8*)p; }
DI f32x16 mfma32(bf16x8 a, bf16x8 b, f32x16 c) { return __builtin_amdgcn_mfma_f32_32x32x16_bf16(a, b, c, 0, 0, 0); }
DI f32x16 mfma32k8(bf16x4 a, bf16x4 b, f32x16 c) { return __builtin_amdgcn_mfma_f32_32x32x8bf16_1k(a, b, c, 0, 0, 0); }
DI f32x4 mfma16(bf16x8 a, bf16x8 b, f32x4 c) { return __builtin_amdgcn_mfma_f32_16x16x32_bf16(a, b, c, 0, 0, 0); }
DI float dot2(unsigned a, unsigned b, float c) {
    return __builtin_amdgcn_fdot2_f32_bf16(__builtin_bit_cast(bf16x2v, a), __builtin_bit_cast(bf16x2v, b), c, false);
}
DI float xhalf_sum(float v) {
    auto r = __builtin_amdgcn_permlane32_swap(__float_as_uint(v), __float_as_uint(v), false, false);
    return __uint_as_float(r[0]) + __uint_as_float(r[1]);
}
DI float xhalf_max(float v) {
    auto r = __builtin_amdgcn_permlane32_swap(__float_as_uint(v), __float_as_uint(v), false, false);
    return __builtin_fmaxf(__uint_as_float(r[0]), __uint_as_float(r[1]));
}
DI int uni(int v) { return __builtin_amdgcn_readfirstlane(v); }
#define PIN(x) asm volatile("" : "+v"(x))
#define SBAR() __builtin_amdgcn_sched_barrier(0)
#define ST_AUX 0
DI void st64(rsrc_t rs, unsigned off, u32x2 v) { __builtin_amdgcn_raw_buffer_store_b64(v, rs, off, 0, ST_AUX); }
DI void st128(rsrc_t rs, unsigned off, u32x4 v) { __builtin_amdgcn_raw_buffer_store_b128(v, rs, off, 0, ST_AUX); }
DI u32x2 ld64g(rsrc_t rs, unsigned off) { return __builtin_bit_cast(u32x2, __builtin_amdgcn_raw_buffer_load_b64(rs, off, 0, 0)); }
DI float rbf(float v) { return bf_lo(cvtpk(v, 0.f)); }
DI void ln_scalars(float s, float ss, float& mean, float& rstd) {
    const float m = s * (1.f / DM), var = fmaxf(ss * (1.f / DM) - m * m, 0.f);
    mean = rbf(m);
    rstd = __builtin_amdgcn_rcpf(rbf(__builtin_amdgcn_sqrtf(rbf(rbf(var) + EPS))));
}
#define PROF_ARG
#define PROF_PASS
#define PROF_DECL
#define MARK(ph, i)
#define PROF_END(ph)

template <int NP, int NT, class Issue, class Work> DI void ring_loader(Issue issue, Work work) {
    static_assert(NT >= 5, "ring depth");
    issue(0); issue(1);
    wait_bar<NP>();
    issue(2); issue(3);
#pragma unroll 1
    for (int t = 0; t + 1 < NT; ++t) {
        work(t);
        if (t + 3 < NT) wait_bar<2 * NP>(); else if (t + 2 < NT) wait_bar<NP>(); else wait_bar<0>();
        if (t + 4 < NT) issue(t + 4);
    }
    work(NT - 1);
}
template <int NP, int NT, class Issue> DI void ring_loader(Issue issue) { ring_loader<NP, NT>(issue, [](int) {}); }

struct Params {
    const uint16_t* x; const uint16_t* wqkv; const uint16_t* wo; const uint16_t* w1; const uint16_t* b1; const uint16_t* w2; const uint16_t* b2;
    uint16_t* out; char* ws;
};

constexpr int A_STAGE = 32768, A_NT = 10, A_STAT = A_STAGE * 4;
constexpr int A_LDS = A_STAT + 2048 + 4 * 256;

template <bool ISV>
DI void phaseA_body(const Params& p, LAS char* sm, int rb, int ct PROF_ARG) {
    const int tid = threadIdx.x, l = tid & 63, w = uni(tid >> 6), r = l & 31, h = l >> 5;
    const int row0 = rb * 128, col0 = ct * 128;
    const unsigned sm0 = (unsigned)(uintptr_t)sm;
    LAS float* sa = (LAS float*)(sm + A_STAT);
    LAS float* sb = sa + 128;
    LAS float* cst = sa + 256;
    MARK(0, 0);
    if (w >= 4) {
        const bool isX = w < 6;
        const rsrc_t rs = mk_rsrc(isX ? (const void*)p.x : (const void*)p.wqkv, isX ? SEQ * DM * 2 : DM * 3 * DM * 2);
        unsigned voff[2];
#pragma unroll
        for (int j = 0; j < 2; ++j) {
            const int u = (w & 1) * 8 + j;
            const int ra = 8 * u + (l >> 3), ca = (l & 7) ^ ((ra >> 1) & 7);
            const int kb = 4 * u + (l >> 4), cb = (l & 15) ^ ((kb & 3) << 2);
            voff[j] = isX ? (unsigned)((row0 + ra) * (DM * 2) + ca * 16) : (unsigned)(kb * (3 * DM * 2) + col0 * 2 + cb * 16);
        }
        const unsigned kstep = isX ? 128u : 64u * (3 * DM * 2), pstep = isX ? 16u * (DM * 2) : 8u * (3 * DM * 2);
        const unsigned ldst = sm0 + (w - 4) * 8192;
        ring_loader<8, A_NT>([&](int t) {
#pragma unroll
            for (int j = 0; j < 8; ++j) dma16(ldst + (t & 3) * A_STAGE + j * 1024, voff[j & 1], rs, kstep * t + pstep * (j >> 1));
        });
        lds_bar();
        return;
    }
    const int wm = w >> 1, wn = w & 1;
    const int tb0 = 64 * wm + 32 * wn, tb1 = tb0 ^ 32, cb0 = 64 * wn + 32 * wm, cb1 = cb0 ^ 32;
    const int g = l >> 4, q = (l & 15) >> 2, p4 = l & 3;
    unsigned xa[4];
#pragma unroll
    for (int ks = 0; ks < 4; ++ks) xa[ks] = ((tb0 + r) * 128 + ((h ^ ((r >> 1) & 7)) << 4)) ^ (ks << 5);
    const unsigned wa = 16384 + (8 * (g >> 1) + q) * 256 + ((((cb0 >> 3) + 2 * (g & 1) + (p4 >> 1)) ^ (q << 2)) << 4) + (p4 & 1) * 8;
    f32x16 acc[2][2] = {};
    struct Frag { bf16x8 x0, x1, w0, w1; } F0, F1;
    auto rd = [&](Frag& F, const LAS char* st, int ks) {
        F.x0 = ld128(st + xa[ks]);
        F.w0 = ldtr2(st + wa + ks * 4096, st + wa + ks * 4096 + 1024);
        F.x1 = ld128(st + (xa[ks] ^ 4096));
        F.w1 = ldtr2(st + (wa ^ 64) + ks * 4096, st + (wa ^ 64) + ks * 4096 + 1024);
        SBAR();
    };
    auto mf = [&](bf16x8 x, bf16x8 wv, f32x16 c) { return ISV ? mfma32(x, wv, c) : mfma32(wv, x, c); };
    float xs0 = 0.f, xs1 = 0.f, xq0 = 0.f, xq1 = 0.f;
    f32x16 csum = {};
    const bf16x8 ones = __builtin_bit_cast(bf16x8, u32x4{0x3f803f80u, 0x3f803f80u, 0x3f803f80u, 0x3f803f80u});
    auto st2 = [&](unsigned u) {
        float lo, hi;
        asm volatile("v_lshlrev_b32 %0, 16, %6\n\tv_and_b32 %1, 0xffff0000, %6\n\tv_add_f32 %2, %2, %0\n\tv_add_f32 %3, %3, %1\n\tv_fmac_f32 %4, %0, %0\n\tv_fmac_f32 %5, %1, %1"
                     : "=&v"(lo), "=&v"(hi), "+v"(xs0), "+v"(xs1), "+v"(xq0), "+v"(xq1) : "v"(u));
        SBAR();
    };
    auto mm = [&](const Frag& F) {
        const u32x4 xu = __builtin_bit_cast(u32x4, F.x0);
        acc[0][0] = mf(F.x0, F.w0, acc[0][0]); SBAR(); st2(xu[0]);
        acc[1][0] = mf(F.x1, F.w0, acc[1][0]); SBAR(); st2(xu[1]);
        csum = mf(ones, F.w0, csum); SBAR(); st2(xu[2]);
        acc[0][1] = mf(F.x0, F.w1, acc[0][1]); SBAR(); st2(xu[3]);
        acc[1][1] = mf(F.x1, F.w1, acc[1][1]); SBAR();
    };
    lds_bar();
    MARK(0, 1);
    rd(F0, sm, 0);
#pragma unroll
    for (int t = 0; t < A_NT; ++t) {
        const LAS char* st = sm + (t & 3) * A_STAGE;
        rd(F1, st, 1); mm(F0);
        rd(F0, st, 2); mm(F1);
        rd(F1, st, 3); mm(F0);
        if (t + 1 < A_NT) { lds_bar(); rd(F0, sm + ((t + 1) & 3) * A_STAGE, 0); }
        mm(F1);
    }
    asm volatile("s_waitcnt vmcnt(0)" ::: "memory");
    PIN(csum);
    MARK(0, 2);
    {
        float mean, ra;
        ln_scalars(xhalf_sum(xs0 + xs1), xhalf_sum(xq0 + xq1), mean, ra);
        if (!ISV && ct < 5) ra *= QSCALE;
        if (h == 0) { sa[tb0 + r] = ra; sb[tb0 + r] = -mean * ra; }
        if (ISV) { if (h == 0) cst[cb0 + r] = csum[0]; }
        else if (r == 0) {
#pragma unroll
            for (int gg = 0; gg < 4; ++gg) *(LAS f32x4*)(cst + cb0 + 8 * gg + 4 * h) = f32x4{csum[4 * gg], csum[4 * gg + 1], csum[4 * gg + 2], csum[4 * gg + 3]};
        }
    }
    lds_bar();
    const rsrc_t rw = mk_rsrc(p.ws, (unsigned)WS_END);
    if (ISV) {
#pragma unroll
        for (int a = 0; a < 2; ++a) {
            const int tb = a ? tb1 : tb0, T = rb * 4 + (tb >> 5);
            f32x4 av[4], bv[4];
#pragma unroll
            for (int gg = 0; gg < 4; ++gg) { av[gg] = *(const LAS f32x4*)(sa + tb + 8 * gg + 4 * h); bv[gg] = *(const LAS f32x4*)(sb + tb + 8 * gg + 4 * h); }
#pragma unroll
            for (int j = 0; j < 2; ++j) {
                const int cl = (j ? cb1 : cb0) + r;
                const float cs = cst[cl];
                const int c = col0 + cl - 2 * DM, head = c / 20, d = c - head * 20;
                const unsigned dst = (unsigned)WS_KV + (head * 64 + T) * 3072 + 1536 + h * 384 + d * 16;
#pragma unroll
                for (int s = 0; s < 2; ++s) {
                    u32x4 v;
#pragma unroll
                    for (int i = 0; i < 4; ++i) {
                        const int e = 8 * s + 2 * i;
                        v[i] = cvtpk(av[e >> 2][e & 3] * acc[a][j][e] + bv[e >> 2][e & 3] * cs, av[e >> 2][(e & 3) + 1] * acc[a][j][e + 1] + bv[e >> 2][(e & 3) + 1] * cs);
                    }
                    st128(rw, dst + s * 768, v);
                    if (d >= 16) {
                        const unsigned c1 = d == 16 ? 0x3f803f80u : 0u;
                        st128(rw, dst + s * 768 + 64, u32x4{c1, c1, c1, c1});
                    }
                }
            }
        }
    } else {
        const bool isQ = ct < 5;
        const float ra0 = sa[tb0 + r], rb0 = sb[tb0 + r], ra1 = sa[tb1 + r], rb1 = sb[tb1 + r];
#pragma unroll
        for (int j = 0; j < 2; ++j)
#pragma unroll
            for (int gg = 0; gg < 4; ++gg) {
                const int cl = (j ? cb1 : cb0) + 8 * gg + 4 * h;
                const f32x4 cs = *(const LAS f32x4*)(cst + cl);
                const int c4 = (col0 + cl - (isQ ? 0 : DM)) >> 2, head = c4 / 5, grp = c4 - head * 5;
                const unsigned in0 = ((grp >> 1) * 32 + r) * 16 + (grp & 1) * 8;
#pragma unroll
                for (int a = 0; a < 2; ++a) {
                    const float ra = a ? ra1 : ra0, rbb = a ? rb1 : rb0;
                    const int T = rb * 4 + ((a ? tb1 : tb0) >> 5);
                    u32x2 v;
                    v[0] = cvtpk(ra * acc[a][j][4 * gg] + rbb * cs[0], ra * acc[a][j][4 * gg + 1] + rbb * cs[1]);
                    v[1] = cvtpk(ra * acc[a][j][4 * gg + 2] + rbb * cs[2], ra * acc[a][j][4 * gg + 3] + rbb * cs[3]);
                    if (isQ) {
                        st64(rw, grp < 4 ? (unsigned)WS_Q0 + (head * 64 + T) * 1024 + in0 : (unsigned)WS_Q1 + (head * SEQ + T * 32 + r) * 8, v);
                    } else {
                        const unsigned base = (unsigned)WS_KV + (head * 64 + T) * 3072;
                        st64(rw, base + (grp < 4 ? in0 : 1024 + r * 8), v);
                        if (grp == 4) st64(rw, base + 1280 + r * 8, u32x2{0x3f803f80u, 0x00003f80u});
                    }
                }
            }
    }
}
DI void phaseA(const Params& p, LAS char* sm, int b PROF_ARG) {
    const int g = b & 7, i = b >> 3, rb = 4 * (g & 3) + (i & 3), ct = (g >> 2) * 8 + (i >> 2);
    {
        const int l = threadIdx.x & 63, w = uni(threadIdx.x >> 6);
        if (w < 4) {
            const unsigned scr = (unsigned)(uintptr_t)sm + A_STAT + 2048 + w * 256;
            touch(scr, b * 10240 + w * 2560 + l * 64, mk_rsrc(p.x, SEQ * DM * 2));
            touch(scr, b * 9600 + w * 2400 + l * 64, mk_rsrc(p.wqkv, DM * 3 * DM * 2));
        }
    }
    if (ct >= 15) { asm volatile("s_waitcnt vmcnt(0)" ::: "memory"); return; }
    if (ct >= 10) phaseA_body<true>(p, sm, rb, ct PROF_PASS); else phaseA_body<false>(p, sm, rb, ct PROF_PASS);
    MARK(0, 3);
}

constexpr int B_STAGE = 8 * 3072, B_NS = 3, B_NT = 8, B_FLAG = B_STAGE * B_NS;
constexpr int B_LDS = B_FLAG + 64 + 8 * 1024;

DI u32x2 neg_split3(float m) {
    const unsigned hi = cvtpk(m, 0.f) & 0xffffu; const float r1 = m - bf_lo(hi);
    const unsigned mid = cvtpk(r1, 0.f) & 0xffffu; const float r2 = r1 - bf_lo(mid);
    const unsigned lo = cvtpk(r2, 0.f) & 0xffffu;
    return u32x2{(hi | (mid << 16)) ^ 0x80008000u, lo ^ 0x8000u};
}
DI float max16(const f32x16& s) {
    float a = __builtin_fmaxf(__builtin_fmaxf(s[0], s[1]), s[2]), b = __builtin_fmaxf(__builtin_fmaxf(s[3], s[4]), s[5]);
    a = __builtin_fmaxf(__builtin_fmaxf(a, s[6]), s[7]); b = __builtin_fmaxf(__builtin_fmaxf(b, s[8]), s[9]);
    a = __builtin_fmaxf(__builtin_fmaxf(a, s[10]), s[11]); b = __builtin_fmaxf(__builtin_fmaxf(b, s[12]), s[13]);
    a = __builtin_fmaxf(__builtin_fmaxf(a, s[14]), s[15]);
    return __builtin_fmaxf(a, b);
}
DI bf16x4 ld64(const LAS char* p) { return *(const LAS bf16x4*)p; }

DI void phaseB(const Params& p, LAS char* sm, int b PROF_ARG) {
    const int tid = threadIdx.x, l = tid & 63, w = uni(tid >> 6), r = l & 31, h = l >> 5;
    const int g = b & 7, i = b >> 3, head = 4 * g + (i >> 3);
    const int kh = w >> 2, TA = (i & 7) * 8 + (w & 3) * 2;
    const unsigned sm0 = (unsigned)(uintptr_t)sm;
    const rsrc_t rs = mk_rsrc(p.ws + WS_KV + (size_t)head * 64 * 3072, 64 * 3072);
    const unsigned voff = (kh * 32 + (w & 3)) * 3072 + l * 16;
    auto issue = [&](int t, int slot) {
#pragma unroll
        for (int j = 0; j < 3; ++j) dma16(sm0 + slot * B_STAGE + w * 3072 + j * 1024, voff, rs, t * 4 * 3072 + j * 1024);
    };
    const rsrc_t rpf = mk_rsrc(w < 3 ? p.w1 : w < 6 ? p.w2 : p.wo, w < 6 ? SEQ * FF * 2 : DM * DM * 2);
    const unsigned pfo = (w < 6 ? b * 24576 + (w % 3) * 8192 : b * 3200) + l * 16, pfl = sm0 + B_FLAG + 64 + w * 1024;
    MARK(1, 0);
    issue(0, 0); issue(1, 1);
    const char* qp0 = p.ws + WS_Q0 + ((size_t)(head * 64 + TA) * 64 + l) * 16;
    const char* qp1 = p.ws + WS_Q1 + ((size_t)head * SEQ + TA * 32 + r) * 8;
    bf16x8 qA0 = *(const bf16x8*)qp0, qB0 = *(const bf16x8*)(qp0 + 1024);
    u32x2 qA1u = *(const u32x2*)qp1, qB1u = *(const u32x2*)(qp1 + 256);
    PIN(qA0); PIN(qB0); PIN(qA1u); PIN(qB1u);
    if (h) { qA1u = u32x2{0u, 0u}; qB1u = u32x2{0u, 0u}; }
    const unsigned k0o = l * 16, k1o = 1024 + l * 8, vo = 1536 + h * 384 + (r < 22 ? r : 22) * 16;
    const LAS char* sw = sm + kh * 4 * 3072;
    f32x16 oA, oB;
    float lsum, mA, mB;
    auto scores = [&](const LAS char* tp, bf16x8 q0, u32x2 q1) {
        return mfma32k8(ld64(tp + k1o), __builtin_bit_cast(bf16x4, q1), mfma32(ld128(tp + k0o), q0, f32x16{}));
    };
    for (int pass = 0;; ++pass) {
        if (pass == 1) {
            if (h) { qA1u = u32x2{0u, 0u}; qB1u = u32x2{0u, 0u}; }
            mA = -INFINITY; mB = -INFINITY;
            issue(0, 0); issue(1, 1);
            for (int t = 0; t < B_NT; ++t) {
                if (t + 1 < B_NT) wait_bar<3>(); else wait_bar<0>();
                if (t + 2 < B_NT) issue(t + 2, (t + 2) % B_NS);
                const LAS char* st = sm + (t % B_NS) * B_STAGE;
#pragma unroll 1
                for (int kt = 0; kt < 8; ++kt) {
                    mA = __builtin_fmaxf(mA, max16(scores(st + kt * 3072, qA0, qA1u)));
                    mB = __builtin_fmaxf(mB, max16(scores(st + kt * 3072, qB0, qB1u)));
                }
            }
            mA = xhalf_max(mA); mB = xhalf_max(mB);
            lds_bar();
            if (h) { qA1u = neg_split3(mA); qB1u = neg_split3(mB); }
            issue(0, 0); issue(1, 1);
        }
        wait_bar<3>();
        dma16(pfl, pfo, rpf, 0);
        if (pass == 0) {
            const f32x16 s0 = scores(sw, qA0, qA1u), s1 = scores(sw, qB0, qB1u);
            mA = xhalf_max(max16(s0)); mB = xhalf_max(max16(s1));
            if (h) { qA1u = neg_split3(mA); qB1u = neg_split3(mB); }
        }
        MARK(1, 1);
        const bf16x4 qA1 = __builtin_bit_cast(bf16x4, qA1u), qB1 = __builtin_bit_cast(bf16x4, qB1u);
        oA = f32x16{}; oB = f32x16{};
        bf16x8 kf0 = ld128(sw + k0o), vfa = ld128(sw + vo), vfb, vf1 = bf16x8{};
        bf16x4 kf1 = ld64(sw + k1o);
        f32x16 sA = mfma32k8(kf1, qA1, mfma32(kf0, qA0, f32x16{})), sB;
        u32x4 p0, p1 = u32x4{0u, 0u, 0u, 0u};
#define EX(v) __builtin_amdgcn_exp2f(v)
#define BF(P) __builtin_bit_cast(bf16x8, P)
#define GRP(S, a, P, c) do { \
            const float e0_ = EX(S[a]), e1_ = EX(S[a + 1]), e2_ = EX(S[a + 2]), e3_ = EX(S[a + 3]); \
            P[c] = cvtpk(e0_, e1_); P[c + 1] = cvtpk(e2_, e3_); PIN(P); SBAR(); } while (0)
#define MF(stmt) do { stmt; SBAR(); } while (0)
#define TILE(TC, TN, V0, V0N) do { \
            MF(sB = mfma32(kf0, qB0, f32x16{}));          GRP(sA, 0, p0, 0); \
            MF(oB = mfma32(vf1, BF(p1), oB)); \
            MF((kf0 = ld128((TN) + k0o), vf1 = ld128((TC) + vo + 768), V0N = ld128((TN) + vo))); \
                                                          GRP(sA, 4, p0, 2); \
            MF(sB = mfma32k8(kf1, qB1, sB)); \
            MF(kf1 = ld64((TN) + k1o));                   GRP(sA, 8, p1, 0); \
            MF(oA = mfma32(V0, BF(p0), oA));              GRP(sA, 12, p1, 2); \
            asm volatile("" : "+v"(kf0), "+v"(kf1), "+v"(vf1), "+v"(V0N)); SBAR(); \
            MF(sA = mfma32(kf0, qA0, f32x16{}));          GRP(sB, 0, p0, 0); \
            MF(oA = mfma32(vf1, BF(p1), oA));             GRP(sB, 4, p0, 2); \
            MF(sA = mfma32k8(kf1, qA1, sA));              GRP(sB, 8, p1, 0); \
            MF(oB = mfma32(V0, BF(p0), oB));              GRP(sB, 12, p1, 2); } while (0)
#pragma unroll
        for (int t = 0; t < B_NT; ++t) {
            const LAS char* st = sw + (t % B_NS) * B_STAGE;
            const LAS char* sn = sw + ((t + 1) % B_NS) * B_STAGE;
            TILE(st, st + 1 * 3072, vfa, vfb);
            TILE(st + 1 * 3072, st + 2 * 3072, vfb, vfa);
            TILE(st + 2 * 3072, st + 3 * 3072, vfa, vfb);
            if (t + 1 < B_NT) {
                wait_bar<1>();
                if (t + 2 < B_NT) issue(t + 2, (t + 2) % B_NS);
                dma16(pfl, pfo, rpf, (t + 1) * 1024);
            } else wait_bar<0>();
            TILE(st + 3 * 3072, sn, vfb, vfa);
        }
        oB = mfma32(vf1, BF(p1), oB);
#undef TILE
#undef MF
#undef GRP
#undef BF
#undef EX
        MARK(1, 2);
        lds_bar();
        LAS f32x4* red = (LAS f32x4*)sm + (w & 3) * 7 * 64 + l;
        LAS float* mex = (LAS float*)((LAS f32x4*)sm + ((w & 3) * 7 + 6) * 64);
        f32x16 o = kh ? oB : oA;
        {
            const f32x16 give = kh ? oA : oB;
#pragma unroll
            for (int e = 0; e < 3; ++e) red[(kh * 3 + e) * 64] = f32x4{give[4 * e], give[4 * e + 1], give[4 * e + 2], give[4 * e + 3]};
            mex[kh * 64 + l] = kh ? mA : mB;
        }
        lds_bar();
        {
            const float mo = mex[(kh ^ 1) * 64 + l], mm = kh ? mB : mA, mx = __builtin_fmaxf(mo, mm);
            const float fo = __builtin_amdgcn_exp2f(mo - mx), fm = __builtin_amdgcn_exp2f(mm - mx);
#pragma unroll
            for (int e = 0; e < 3; ++e) {
                const f32x4 v = red[((kh ^ 1) * 3 + e) * 64];
#pragma unroll
                for (int k = 0; k < 4; ++k) o[4 * e + k] = o[4 * e + k] * fm + v[k] * fo;
            }
        }
        oA = o;
        {
            auto rr = __builtin_amdgcn_permlane32_swap(__float_as_uint(o[8]), __float_as_uint(o[8]), false, false);
            lsum = __uint_as_float(rr[1]);
        }
        if (pass == 1) break;
        bool ok = lsum > 0x1p-30f && lsum < 0x1p64f;
#pragma unroll
        for (int e = 0; e < 12; ++e) ok = ok && (e >= 8 && h ? true : __builtin_fabsf(o[e]) < INFINITY);
        LAS int* flag = (LAS int*)(sm + B_FLAG);
        const bool wave_ok = __all(ok);
        if (l == 0) flag[w] = wave_ok ? 0 : 1;
        lds_bar();
        const u32x4 f0 = *(LAS u32x4*)flag, f1 = *(LAS u32x4*)(flag + 4);
        if (!uni(f0[0] | f0[1] | f0[2] | f0[3] | f1[0] | f1[1] | f1[2] | f1[3])) break;
        lds_bar();
    }
    const float inv = __builtin_amdgcn_rcpf(lsum);
    const rsrc_t rw = mk_rsrc(p.ws, (unsigned)WS_END);
    const unsigned orow = (unsigned)WS_O + ((TA + kh) * 32 + r) * (DM * 2);
#pragma unroll
    for (int e = 0; e < 3; ++e) {
        if (e < 2 || h == 0) {
            const int gam = head * 5 + 2 * e + h;
            const int G = gam & 7, pos = (gam & ~7) + 2 * (G & 3) + (G >> 2);
            u32x2 v;
            v[0] = cvtpk(oA[4 * e] * inv, oA[4 * e + 1] * inv); v[1] = cvtpk(oA[4 * e + 2] * inv, oA[4 * e + 3] * inv);
            st64(rw, orow + pos * 8, v);
        }
    }
    MARK(1, 3);
}

constexpr int C_STAGE = 36864, C_NT = 5, C_RED = C_STAGE * 4;
constexpr int C_LDS = C_RED + 8 * 64 * 8 + 64;

DI void phaseC(const Params& p, LAS char* sm, int b PROF_ARG) {
    const int tid = threadIdx.x, l = tid & 63, w = uni(tid >> 6);
    const int gx = b & 7, ix = b >> 3, rb = 4 * gx + (ix >> 3), cb = ix & 7;
    const int row0 = rb * 64, col0 = cb * 80;
    const unsigned sm0 = (unsigned)(uintptr_t)sm;
    const int tk = l & 15, g = l >> 4, q = (l & 15) >> 2, p4 = l & 3;
    const int row = row0 + 16 * (w & 3) + tk;
    const rsrc_t rw = mk_rsrc(p.ws, (unsigned)WS_END);
    float hv[20];
    unsigned long long* gran = (unsigned long long*)(p.ws + WS_ST) + (size_t)rb * 8 * 64 * 2;
    MARK(2, 0);
    if (w >= 4) {
        const int lw = w - 4;
        const rsrc_t rsO = mk_rsrc(p.ws + WS_O, SEQ * DM * 2), rsW = mk_rsrc(p.wo, DM * DM * 2);
        unsigned vo[4], vw[5];
#pragma unroll
        for (int j = 0; j < 4; ++j) { const int rr = 4 * (lw * 4 + j) + (l >> 4), c = (l & 15) ^ (rr & 15); vo[j] = (row0 + rr) * (DM * 2) + c * 16; }
#pragma unroll
        for (int j = 0; j < 5; ++j) { const int ch = (lw * 5 + j) * 64 + l, kr = ch / 10, c = ch - kr * 10; vw[j] = kr * (DM * 2) + col0 * 2 + c * 16; }
        ring_loader<9, C_NT>([&](int t) {
            const unsigned base = sm0 + (t & 3) * C_STAGE;
#pragma unroll
            for (int j = 0; j < 4; ++j) dma16(base + (lw * 4 + j) * 1024, vo[j], rsO, t * 256);
#pragma unroll
            for (int j = 0; j < 5; ++j) dma16(base + 16384 + (lw * 5 + j) * 1024, vw[j], rsW, t * 128 * (DM * 2));
        });
    } else {
        u32x2 xv[5];
        {
            const rsrc_t rx = mk_rsrc(p.x, SEQ * DM * 2);
#pragma unroll
            for (int j = 0; j < 5; ++j) xv[j] = ld64g(rx, (row * DM + col0 + 16 * j + 4 * g) * 2);
        }
        unsigned oa[4];
#pragma unroll
        for (int ks = 0; ks < 4; ++ks) oa[ks] = (16 * w + tk) * 256 + (((4 * ks + g) ^ tk) << 4);
        const unsigned wa = 16384 + (4 * g + q) * 160 + p4 * 8;
        f32x4 acc[5] = {};
        struct Frag { bf16x8 o, w[5]; } F0, F1;
        auto rd = [&](Frag& F, const LAS char* st, int ks) {
            F.o = ld128(st + oa[ks]);
#pragma unroll
            for (int j = 0; j < 5; ++j) F.w[j] = ldtr2(st + wa + ks * 32 * 160 + j * 32, st + wa + ks * 32 * 160 + j * 32 + 16 * 160);
            SBAR();
        };
        auto mm = [&](const Frag& F) {
#pragma unroll
            for (int j = 0; j < 5; ++j) acc[j] = mfma16(F.w[j], F.o, acc[j]);
            SBAR();
        };
        lds_bar();
        MARK(2, 1);
        rd(F0, sm, 0);
#pragma unroll
        for (int t = 0; t < C_NT; ++t) {
            const LAS char* st = sm + (t & 3) * C_STAGE;
            rd(F1, st, 1); mm(F0);
            rd(F0, st, 2); mm(F1);
            rd(F1, st, 3); mm(F0);
            if (t + 1 < C_NT) { lds_bar(); rd(F0, sm + ((t + 1) & 3) * C_STAGE, 0); }
            mm(F1);
        }
        MARK(2, 2);
        float s = 0.f, ss = 0.f;
        u32x2 hb[5];
#pragma unroll
        for (int j = 0; j < 5; ++j) {
            const f32x4 a = acc[j];
            hb[j][0] = cvtpk(a[0] + bf_lo(xv[j][0]), a[1] + bf_hi(xv[j][0])); hb[j][1] = cvtpk(a[2] + bf_lo(xv[j][1]), a[3] + bf_hi(xv[j][1]));
            hv[4 * j] = bf_lo(hb[j][0]); hv[4 * j + 1] = bf_hi(hb[j][0]); hv[4 * j + 2] = bf_lo(hb[j][1]); hv[4 * j + 3] = bf_hi(hb[j][1]);
#pragma unroll
            for (int e = 0; e < 4; ++e) { s += hv[4 * j + e]; ss += hv[4 * j + e] * hv[4 * j + e]; }
        }
        s += __shfl_xor(s, 16); ss += __shfl_xor(ss, 16); s = xhalf_sum(s); ss = xhalf_sum(ss);
        if (g < 2) {
            const unsigned long long v = (1ull << 32) | __float_as_uint(g == 0 ? s : ss);
            __hip_atomic_store(gran + ((size_t)cb * 64 + 16 * w + tk) * 2 + g, v, __ATOMIC_RELAXED, __HIP_MEMORY_SCOPE_AGENT);
        }
#pragma unroll
        for (int j = 0; j < 5; ++j) st64(rw, (unsigned)WS_H + (row * DM + col0 + 16 * j + 4 * g) * 2, hb[j]);
    }
    MARK(2, 3);
    LAS float* fin = (LAS float*)(sm + C_RED);
    {
        unsigned long long a, c; unsigned spins = 0;
        for (;;) {
            a = __hip_atomic_load(gran + ((size_t)w * 64 + l) * 2, __ATOMIC_RELAXED, __HIP_MEMORY_SCOPE_AGENT);
            c = __hip_atomic_load(gran + ((size_t)w * 64 + l) * 2 + 1, __ATOMIC_RELAXED, __HIP_MEMORY_SCOPE_AGENT);
            if (__all((a >> 32) == 1ull && (c >> 32) == 1ull)) break;
            if (++spins > (1u << 20)) break;
            __builtin_amdgcn_s_sleep(1);
        }
        *(LAS u32x2*)(fin + (w * 64 + l) * 2) = u32x2{(unsigned)a, (unsigned)c};
    }
    lds_bar();
    MARK(2, 4);
    if (w < 4) {
        float s = 0.f, ss = 0.f;
#pragma unroll
        for (int j = 0; j < 8; ++j) { const u32x2 v = *(const LAS u32x2*)(fin + (j * 64 + 16 * w + tk) * 2); s += __uint_as_float(v[0]); ss += __uint_as_float(v[1]); }
        const float m = s * (1.f / DM), var = fmaxf(ss * (1.f / DM) - m * m, 0.f);
        const float r1 = __builtin_amdgcn_rsqf(var + EPS);
        const float r2 = __builtin_amdgcn_rsqf(var * r1 * r1 + EPS), rr = r1 * r2;
#pragma unroll
        for (int j = 0; j < 5; ++j) {
            u32x2 v;
            v[0] = cvtpk((hv[4 * j] - m) * rr, (hv[4 * j + 1] - m) * rr); v[1] = cvtpk((hv[4 * j + 2] - m) * rr, (hv[4 * j + 3] - m) * rr);
            st64(rw, (unsigned)WS_HN + (row * DM + col0 + 16 * j + 4 * g) * 2, v);
        }
    }
    MARK(2, 5);
}

constexpr int D_STAGE = 32768, D_NT = 16;
constexpr int D_LDS = D_STAGE * 4;

DI float gelu_tanh(float v) {
    const float y = v * (0.7978845608028654f + 0.7978845608028654f * 0.044715f * v * v);
    return v * __builtin_amdgcn_rcpf(1.f + __builtin_amdgcn_exp2f(-2.f * 1.4426950408889634f * y));
}
DI void phaseD(const Params& p, LAS char* sm, int b PROF_ARG) {
    const int gx = b & 7, ix = b >> 3;
    if (ix >= 30) return;
    const int db = 5 * (gx & 1) + ix % 5, fb = 6 * (gx >> 1) + ix / 5;
    const int tid = threadIdx.x, l = tid & 63, w = uni(tid >> 6);
    const unsigned sm0 = (unsigned)(uintptr_t)sm;
    MARK(3, 0);
    if (w >= 4) {
        const bool isH = w < 6;
        const int pitch = isH ? DM * 2 : FF * 2;
        const rsrc_t rs = mk_rsrc(isH ? (const void*)(p.ws + WS_HN) : (const void*)p.w1, isH ? SEQ * DM * 2 : SEQ * FF * 2);
        const int kr = 64 * (w & 1) + (l >> 3);
        const unsigned voff = kr * pitch + (isH ? db : fb) * 128 + (((l & 7) ^ (((kr >> 1) & 1) << 2)) << 4);
        const unsigned ldst = sm0 + (w - 4) * 8192;
        ring_loader<8, D_NT>([&](int t) {
#pragma unroll
            for (int j = 0; j < 8; ++j) dma16(ldst + (t & 3) * D_STAGE + j * 1024, voff, rs, (unsigned)((t * 128 + j * 8) * pitch));
        });
        return;
    }
    const int wf = w >> 1, wd = w & 1, r = l & 31, h = l >> 5;
    const int g = l >> 4, q = (l & 15) >> 2, p4 = l & 3;
    u32x2 bv[4];
    {
        const rsrc_t rb1 = mk_rsrc(p.b1, FF * 2);
#pragma unroll
        for (int e = 0; e < 4; ++e) bv[e] = ld64g(rb1, (fb * 64 + 32 * wf + 8 * e + 4 * h) * 2);
    }
    const unsigned rowo = (8 * (g >> 1) + q) * 128 + (p4 & 1) * 8, sw = ((q >> 1) & 1) << 2;
    const unsigned ha = rowo + (((4 * wd + 2 * (g & 1) + (p4 >> 1)) ^ sw) << 4);
    const unsigned wa = 16384 + rowo + (((4 * wf + 2 * (g & 1) + (p4 >> 1)) ^ sw) << 4);
    f32x16 acc = {};
    struct Frag { bf16x8 h, w; } F0, F1;
    auto rd = [&](Frag& F, const LAS char* st, int ks) {
        F.h = ldtr2(st + ha + ks * 2048, st + ha + ks * 2048 + 512);
        F.w = ldtr2(st + wa + ks * 2048, st + wa + ks * 2048 + 512);
        SBAR();
    };
    auto mm = [&](const Frag& F) { acc = mfma32(F.w, F.h, acc); SBAR(); };
    lds_bar();
    MARK(3, 1);
    rd(F0, sm, 0);
#pragma unroll
    for (int t = 0; t < D_NT; ++t) {
        const LAS char* st = sm + (t & 3) * D_STAGE;
        rd(F1, st, 1); mm(F0);
        rd(F0, st, 2); mm(F1);
        rd(F1, st, 3); mm(F0);
        rd(F0, st, 4); mm(F1);
        rd(F1, st, 5); mm(F0);
        rd(F0, st, 6); mm(F1);
        rd(F1, st, 7); mm(F0);
        if (t + 1 < D_NT) { lds_bar(); rd(F0, sm + ((t + 1) & 3) * D_STAGE, 0); }
        mm(F1);
    }
    MARK(3, 2);
    {
        const rsrc_t rw = mk_rsrc(p.ws, (unsigned)WS_END);
        const int d = db * 64 + 32 * wd + r;
#pragma unroll
        for (int e = 0; e < 4; ++e) {
            const int f0 = fb * 64 + 32 * wf + 8 * e + 4 * h;
            u32x2 v;
            v[0] = cvtpk(gelu_tanh(acc[4 * e] + bf_lo(bv[e][0])), gelu_tanh(acc[4 * e + 1] + bf_hi(bv[e][0])));
            v[1] = cvtpk(gelu_tanh(acc[4 * e + 2] + bf_lo(bv[e][1])), gelu_tanh(acc[4 * e + 3] + bf_hi(bv[e][1])));
            st64(rw, (unsigned)WS_U + (d * FF + f0) * 2, v);
        }
    }
    MARK(3, 3);
}

constexpr int E_STAGE = 36864, E_NT = 12;
constexpr int E_LDS = E_STAGE * 4;

DI void phaseE(const Params& p, LAS char* sm, int b PROF_ARG) {
    const int gx = b & 7, ix = b >> 3, db = 4 * (gx & 1) + (ix & 3), sb = 8 * (gx >> 1) + (ix >> 2);
    const int d0 = db * 80, s0 = sb * 64;
    const int tid = threadIdx.x, l = tid & 63, w = uni(tid >> 6);
    const unsigned sm0 = (unsigned)(uintptr_t)sm;
    MARK(4, 0);
    if (w >= 4) {
        const int lw = w - 4;
        const rsrc_t rsU = mk_rsrc(p.ws + WS_U, DM * FF * 2), rsW = mk_rsrc(p.w2, FF * SEQ * 2);
        unsigned vu[5], vw[2];
#pragma unroll
        for (int j = 0; j < 5; ++j) { const int rr = 4 * (lw * 5 + j) + (l >> 4), c = (l & 15) ^ (rr & 15); vu[j] = (d0 + rr) * (FF * 2) + c * 16; }
#pragma unroll
        for (int j = 0; j < 2; ++j) {
            const int kr = 8 * (lw * 4 + j) + (l >> 3), fr = ((kr >> 1) & 1) | (((kr >> 3) & 1) << 1);
            vw[j] = kr * (SEQ * 2) + s0 * 2 + (((l & 7) ^ (fr << 1)) << 4);
        }
        ring_loader<9, E_NT>([&](int t) {
            const unsigned base = sm0 + (t & 3) * E_STAGE;
#pragma unroll
            for (int j = 0; j < 5; ++j) dma16(base + (lw * 5 + j) * 1024, vu[j], rsU, t * 256);
#pragma unroll
            for (int j = 0; j < 4; ++j) dma16(base + 20480 + (lw * 4 + j) * 1024, vw[j & 1], rsW, (unsigned)((t * 128 + (j >> 1) * 16) * (SEQ * 2)));
        });
        return;
    }
    const int tk = l & 15, g = l >> 4, q = (l & 15) >> 2, p4 = l & 3;
    const int srow = s0 + 16 * w + tk;
    u32x2 hv[5];
    unsigned b2u;
    {
        const rsrc_t rh = mk_rsrc(p.ws + WS_H, SEQ * DM * 2), rb2 = mk_rsrc(p.b2, SEQ * 2);
#pragma unroll
        for (int j = 0; j < 5; ++j) hv[j] = ld64g(rh, (srow * DM + d0 + 16 * j + 4 * g) * 2);
        b2u = __builtin_amdgcn_raw_buffer_load_b16(rb2, srow * 2, 0, 0);
    }
    const int fr = ((q >> 1) & 1) | ((g & 1) << 1);
    const unsigned wa = 20480 + (8 * g + q) * 128 + (((2 * w + (p4 >> 1)) ^ (fr << 1)) << 4) + (p4 & 1) * 8;
    unsigned uo[4];
#pragma unroll
    for (int ks = 0; ks < 4; ++ks) uo[ks] = tk * 256 + (((4 * ks + g) ^ tk) << 4);
    f32x4 acc[5] = {};
    struct Frag { bf16x8 w, u[5]; } F0, F1;
    auto rd = [&](Frag& F, const LAS char* st, int ks) {
        F.w = ldtr2(st + wa + ks * 4096, st + wa + ks * 4096 + 512);
#pragma unroll
        for (int j = 0; j < 5; ++j) F.u[j] = ld128(st + uo[ks] + j * 4096);
        SBAR();
    };
    auto mm = [&](const Frag& F) {
#pragma unroll
        for (int j = 0; j < 5; ++j) acc[j] = mfma16(F.u[j], F.w, acc[j]);
        SBAR();
    };
    lds_bar();
    MARK(4, 1);
    rd(F0, sm, 0);
#pragma unroll
    for (int t = 0; t < E_NT; ++t) {
        const LAS char* st = sm + (t & 3) * E_STAGE;
        rd(F1, st, 1); mm(F0);
        rd(F0, st, 2); mm(F1);
        rd(F1, st, 3); mm(F0);
        if (t + 1 < E_NT) { lds_bar(); rd(F0, sm + ((t + 1) & 3) * E_STAGE, 0); }
        mm(F1);
    }
    MARK(4, 2);
    {
        const rsrc_t ro = mk_rsrc(p.out, SEQ * DM * 2);
        const float bias = bf_lo(b2u);
#pragma unroll
        for (int j = 0; j < 5; ++j) {
            const f32x4 a = acc[j];
            u32x2 v;
            v[0] = cvtpk(bf_lo(hv[j][0]) + (a[0] + bias), bf_hi(hv[j][0]) + (a[1] + bias));
            v[1] = cvtpk(bf_lo(hv[j][1]) + (a[2] + bias), bf_hi(hv[j][1]) + (a[3] + bias));
            __builtin_amdgcn_raw_buffer_store_b64(v, ro, (srow * DM + d0 + 16 * j + 4 * g) * 2, 0, 0);
        }
    }
    MARK(4, 3);
}

__global__ void __launch_bounds__(512, 2) k_phaseA(Params p) {
    extern __shared__ __attribute__((aligned(16))) char smem[];
    { const int i = blockIdx.x * 512 + threadIdx.x; if (i < (int)(WS_ST_BYTES / 16)) *(u32x4*)(p.ws + WS_ST + (size_t)i * 16) = u32x4{0u, 0u, 0u, 0u}; }
    PROF_DECL phaseA(p, (LAS char*)smem, blockIdx.x PROF_PASS); PROF_END(0);
}
__global__ void __launch_bounds__(512, 2) k_phaseB(Params p) { extern __shared__ __attribute__((aligned(16))) char smem[]; PROF_DECL phaseB(p, (LAS char*)smem, blockIdx.x PROF_PASS); PROF_END(1); }
__global__ void __launch_bounds__(512, 2) k_phaseC(Params p) { extern __shared__ __attribute__((aligned(16))) char smem[]; PROF_DECL phaseC(p, (LAS char*)smem, blockIdx.x PROF_PASS); PROF_END(2); }
__global__ void __launch_bounds__(512, 2) k_phaseD(Params p) { extern __shared__ __attribute__((aligned(16))) char smem[]; PROF_DECL phaseD(p, (LAS char*)smem, blockIdx.x PROF_PASS); PROF_END(3); }
__global__ void __launch_bounds__(512, 2) k_phaseE(Params p) { extern __shared__ __attribute__((aligned(16))) char smem[]; PROF_DECL phaseE(p, (LAS char*)smem, blockIdx.x PROF_PASS); PROF_END(4); }

extern "C" void kernel_launch(void* const* d_in, const int* in_sizes, int n_in, void* d_out, int out_size, void* d_ws, size_t ws_size,
                              hipStream_t stream) {
    Params p;
    p.x = (const uint16_t*)d_in[0]; p.wqkv = (const uint16_t*)d_in[1]; p.wo = (const uint16_t*)d_in[2];
    p.w1 = (const uint16_t*)d_in[4]; p.b1 = (const uint16_t*)d_in[5]; p.w2 = (const uint16_t*)d_in[6]; p.b2 = (const uint16_t*)d_in[7];
    p.out = (uint16_t*)d_out; p.ws = (char*)d_ws;
    (void)hipFuncSetAttribute((const void*)k_phaseA, hipFuncAttributeMaxDynamicSharedMemorySize, A_LDS);
    (void)hipFuncSetAttribute((const void*)k_phaseB, hipFuncAttributeMaxDynamicSharedMemorySize, B_LDS);
    (void)hipFuncSetAttribute((const void*)k_phaseC, hipFuncAttributeMaxDynamicSharedMemorySize, C_LDS);
    (void)hipFuncSetAttribute((const void*)k_phaseD, hipFuncAttributeMaxDynamicSharedMemorySize, D_LDS);
    (void)hipFuncSetAttribute((const void*)k_phaseE, hipFuncAttributeMaxDynamicSharedMemorySize, E_LDS);
    k_phaseA<<<256, 512, A_LDS, stream>>>(p);
    k_phaseB<<<256, 512, B_LDS, stream>>>(p);
    k_phaseC<<<256, 512, C_LDS, stream>>>(p);
    k_phaseD<<<256, 512, D_LDS, stream>>>(p);
    k_phaseE<<<256, 512, E_LDS, stream>>>(p);
}
```

```cpp
#define X_TLB 65536u
#include <hip/hip_runtime.h>
#include <hip/hip_bf16.h>
#include <stdint.h>

#define LAS __attribute__((address_space(3)))
#define DI __device__ __forceinline__
typedef __attribute__((ext_vector_type(8))) short bf16x8;
typedef __attribute__((ext_vector_type(4))) short bf16x4;
typedef __attribute__((ext_vector_type(2))) __bf16 bf16x2v;
typedef __attribute__((ext_vector_type(16))) float f32x16;
typedef __attribute__((ext_vector_type(4))) float f32x4;
typedef __attribute__((ext_vector_type(4))) unsigned u32x4;
typedef __attribute__((ext_vector_type(2))) unsigned u32x2;
typedef __amdgpu_buffer_rsrc_t rsrc_t;

constexpr int SEQ = 2048, DM = 640, NH = 32, FF = 1536;
constexpr float EPS = 1e-5f;
constexpr float QSCALE = 0.22360679774997896f * 1.4426950408889634f;

constexpr size_t WS_KV = 0;
constexpr size_t WS_Q0 = WS_KV + (size_t)NH * 64 * 3072;
constexpr size_t WS_Q1 = WS_Q0 + (size_t)NH * 64 * 1024;
constexpr size_t WS_O  = WS_Q1 + (size_t)NH * SEQ * 8;
constexpr size_t WS_H  = WS_O + (size_t)SEQ * DM * 2;
constexpr size_t WS_HN = WS_H + (size_t)SEQ * DM * 2;
constexpr size_t WS_U  = WS_HN + (size_t)SEQ * DM * 2;
constexpr size_t WS_ST = WS_U + (size_t)DM * FF * 2;
constexpr size_t WS_ST_BYTES = (size_t)32 * 8 * 64 * 16;
constexpr size_t WS_END = WS_ST + WS_ST_BYTES;

DI rsrc_t mk_rsrc(const void* p, unsigned bytes) { return __builtin_amdgcn_make_buffer_rsrc((void*)p, (short)0, (int)bytes, 0x00020000); }
DI void dma16(unsigned lds, unsigned voff, rsrc_t rs, unsigned soff) {
    asm volatile("s_mov_b32 m0, %0\n\ts_nop 0\n\tbuffer_load_dwordx4 %1, %2, %3 offen lds" ::"s"(lds), "v"(voff), "s"(rs), "s"(soff) : "memory");
}
DI void touch(unsigned lds, unsigned voff, rsrc_t rs) {
    asm volatile("s_mov_b32 m0, %0\n\ts_nop 0\n\tbuffer_load_dword %1, %2, 0 offen lds" ::"s"(lds), "v"(voff), "s"(rs) : "memory");
}
template <int N> DI void wait_bar() { asm volatile("s_waitcnt vmcnt(%0) lgkmcnt(0)\n\ts_barrier" ::"i"(N) : "memory"); }
DI void lds_bar() { asm volatile("s_waitcnt lgkmcnt(0)\n\ts_barrier" ::: "memory"); }
DI unsigned cvtpk(float lo, float hi) {
    typedef __attribute__((ext_vector_type(2))) float f32x2;
    return __builtin_bit_cast(unsigned, __builtin_convertvector(f32x2{lo, hi}, bf16x2v));
}
DI float bf_lo(unsigned u) { return __uint_as_float(u << 16); }
DI float bf_hi(unsigned u) { return __uint_as_float(u & 0xffff0000u); }
DI bf16x4 ldtr(const LAS char* p) { return __builtin_amdgcn_ds_read_tr16_b64_v4i16((LAS bf16x4*)p); }
DI bf16x8 ldtr2(const LAS char* p0, const LAS char* p1) {
    const bf16x4 a = ldtr(p0), b = ldtr(p1);
    return __builtin_shufflevector(a, b, 0, 1, 2, 3, 4, 5, 6, 7);
}
DI bf16x8 ld128(const LAS char* p) { return *(const LAS bf16x8*)p; }
DI f32x16 mfma32(bf16x8 a, bf16x8 b, f32x16 c) { return __builtin_amdgcn_mfma_f32_32x32x16_bf16(a, b, c, 0, 0, 0); }
DI f32x16 mfma32k8(bf16x4 a, bf16x4 b, f32x16 c) { return __builtin_amdgcn_mfma_f32_32x32x8bf16_1k(a, b, c, 0, 0, 0); }
DI f32x4 mfma16(bf16x8 a, bf16x8 b, f32x4 c) { return __builtin_amdgcn_mfma_f32_16x16x32_bf16(a, b, c, 0, 0, 0); }
DI float dot2(unsigned a, unsigned b, float c) {
    return __builtin_amdgcn_fdot2_f32_bf16(__builtin_bit_cast(bf16x2v, a), __builtin_bit_cast(bf16x2v, b), c, false);
}
DI float xhalf_sum(float v) {
    auto r = __builtin_amdgcn_permlane32_swap(__float_as_uint(v), __float_as_uint(v), false, false);
    return __uint_as_float(r[0]) + __uint_as_float(r[1]);
}
DI float xhalf_max(float v) {
    auto r = __builtin_amdgcn_permlane32_swap(__float_as_uint(v), __float_as_uint(v), false, false);
    return __builtin_fmaxf(__uint_as_float(r[0]), __uint_as_float(r[1]));
}
DI int uni(int v) { return __builtin_amdgcn_readfirstlane(v); }
#define PIN(x) asm volatile("" : "+v"(x))
#define SBAR() __builtin_amdgcn_sched_barrier(0)
#define ST_AUX 0
DI void st64(rsrc_t rs, unsigned off, u32x2 v) { __builtin_amdgcn_raw_buffer_store_b64(v, rs, off, 0, ST_AUX); }
DI void st128(rsrc_t rs, unsigned off, u32x4 v) { __builtin_amdgcn_raw_buffer_store_b128(v, rs, off, 0, ST_AUX); }
DI u32x2 ld64g(rsrc_t rs, unsigned off) { return __builtin_bit_cast(u32x2, __builtin_amdgcn_raw_buffer_load_b64(rs, off, 0, 0)); }
DI float rbf(float v) { return bf_lo(cvtpk(v, 0.f)); }
DI void ln_scalars(float s, float ss, float& mean, float& rstd) {
    const float m = s * (1.f / DM), var = fmaxf(ss * (1.f / DM) - m * m, 0.f);
    mean = rbf(m);
    rstd = __builtin_amdgcn_rcpf(rbf(__builtin_amdgcn_sqrtf(rbf(rbf(var) + EPS))));
}
#define PROF_ARG
#define PROF_PASS
#define PROF_DECL
#define MARK(ph, i)
#define PROF_END(ph)

template <int NP, int NT, class Issue, class Work> DI void ring_loader(Issue issue, Work work) {
    static_assert(NT >= 5, "ring depth");
    issue(0); issue(1);
    wait_bar<NP>();
    issue(2); issue(3);
#pragma unroll 1
    for (int t = 0; t + 1 < NT; ++t) {
        work(t);
        if (t + 3 < NT) wait_bar<2 * NP>(); else if (t + 2 < NT) wait_bar<NP>(); else wait_bar<0>();
        if (t + 4 < NT) issue(t + 4);
    }
    work(NT - 1);
}
template <int NP, int NT, class Issue> DI void ring_loader(Issue issue) { ring_loader<NP, NT>(issue, [](int) {}); }

struct Params {
    const uint16_t* x; const uint16_t* wqkv; const uint16_t* wo; const uint16_t* w1; const uint16_t* b1; const uint16_t* w2; const uint16_t* b2;
    uint16_t* out; char* ws;
};

constexpr int A_STAGE = 32768, A_NT = 10, A_STAT = A_STAGE * 4;
constexpr int A_LDS = A_STAT + 2048 + 4 * 256;

template <bool ISV>
DI void phaseA_body(const Params& p, LAS char* sm, int rb, int ct PROF_ARG) {
    const int tid = threadIdx.x, l = tid & 63, w = uni(tid >> 6), r = l & 31, h = l >> 5;
    const int row0 = rb * 128, col0 = ct * 128;
    const unsigned sm0 = (unsigned)(uintptr_t)sm;
    LAS float* sa = (LAS float*)(sm + A_STAT);
    LAS float* sb = sa + 128;
    LAS float* cst = sa + 256;
    MARK(0, 0);
    if (w >= 4) {
        const bool isX = w < 6;
        const rsrc_t rs = mk_rsrc(isX ? (const void*)p.x : (const void*)p.wqkv, isX ? SEQ * DM * 2 : DM * 3 * DM * 2);
        unsigned voff[2];
#pragma unroll
        for (int j = 0; j < 2; ++j) {
            const int u = (w & 1) * 8 + j;
            const int ra = 8 * u + (l >> 3), ca = (l & 7) ^ ((ra >> 1) & 7);
            const int kb = 4 * u + (l >> 4), cb = (l & 15) ^ ((kb & 3) << 2);
            voff[j] = isX ? (unsigned)((row0 + ra) * (DM * 2) + ca * 16) : (unsigned)(kb * (3 * DM * 2) + col0 * 2 + cb * 16);
        }
        const unsigned kstep = isX ? 128u : 64u * (3 * DM * 2), pstep = isX ? 16u * (DM * 2) : 8u * (3 * DM * 2);
        const unsigned ldst = sm0 + (w - 4) * 8192;
        ring_loader<8, A_NT>([&](int t) {
#pragma unroll
            for (int j = 0; j < 8; ++j) dma16(ldst + (t & 3) * A_STAGE + j * 1024, voff[j & 1], rs, kstep * t + pstep * (j >> 1));
        });
        lds_bar();
        return;
    }
    const int wm = w >> 1, wn = w & 1;
    const int tb0 = 64 * wm + 32 * wn, tb1 = tb0 ^ 32, cb0 = 64 * wn + 32 * wm, cb1 = cb0 ^ 32;
    const int g = l >> 4, q = (l & 15) >> 2, p4 = l & 3;
    unsigned xa[4];
#pragma unroll
    for (int ks = 0; ks < 4; ++ks) xa[ks] = ((tb0 + r) * 128 + ((h ^ ((r >> 1) & 7)) << 4)) ^ (ks << 5);
    const unsigned wa = 16384 + (8 * (g >> 1) + q) * 256 + ((((cb0 >> 3) + 2 * (g & 1) + (p4 >> 1)) ^ (q << 2)) << 4) + (p4 & 1) * 8;
    f32x16 acc[2][2] = {};
    struct Frag { bf16x8 x0, x1, w0, w1; } F0, F1;
    auto rd = [&](Frag& F, const LAS char* st, int ks) {
        F.x0 = ld128(st + xa[ks]);
        F.w0 = ldtr2(st + wa + ks * 4096, st + wa + ks * 4096 + 1024);
        F.x1 = ld128(st + (xa[ks] ^ 4096));
        F.w1 = ldtr2(st + (wa ^ 64) + ks * 4096, st + (wa ^ 64) + ks * 4096 + 1024);
        SBAR();
    };
    auto mf = [&](bf16x8 x, bf16x8 wv, f32x16 c) { return ISV ? mfma32(x, wv, c) : mfma32(wv, x, c); };
    float xs0 = 0.f, xs1 = 0.f, xq0 = 0.f, xq1 = 0.f;
    f32x16 csum = {};
    const bf16x8 ones = __builtin_bit_cast(bf16x8, u32x4{0x3f803f80u, 0x3f803f80u, 0x3f803f80u, 0x3f803f80u});
    auto st2 = [&](unsigned u) {
        float lo, hi;
        asm volatile("v_lshlrev_b32 %0, 16, %6\n\tv_and_b32 %1, 0xffff0000, %6\n\tv_add_f32 %2, %2, %0\n\tv_add_f32 %3, %3, %1\n\tv_fmac_f32 %4, %0, %0\n\tv_fmac_f32 %5, %1, %1"
                     : "=&v"(lo), "=&v"(hi), "+v"(xs0), "+v"(xs1), "+v"(xq0), "+v"(xq1) : "v"(u));
        SBAR();
    };
    auto mm = [&](const Frag& F) {
        const u32x4 xu = __builtin_bit_cast(u32x4, F.x0);
        acc[0][0] = mf(F.x0, F.w0, acc[0][0]); SBAR(); st2(xu[0]);
        acc[1][0] = mf(F.x1, F.w0, acc[1][0]); SBAR(); st2(xu[1]);
        csum = mf(ones, F.w0, csum); SBAR(); st2(xu[2]);
        acc[0][1] = mf(F.x0, F.w1, acc[0][1]); SBAR(); st2(xu[3]);
        acc[1][1] = mf(F.x1, F.w1, acc[1][1]); SBAR();
    };
    lds_bar();
    MARK(0, 1);
    rd(F0, sm, 0);
#pragma unroll
    for (int t = 0; t < A_NT; ++t) {
        const LAS char* st = sm + (t & 3) * A_STAGE;
        rd(F1, st, 1); mm(F0);
        rd(F0, st, 2); mm(F1);
        rd(F1, st, 3); mm(F0);
        if (t + 1 < A_NT) { lds_bar(); rd(F0, sm + ((t + 1) & 3) * A_STAGE, 0); }
        mm(F1);
    }
    asm volatile("s_waitcnt vmcnt(0)" ::: "memory");
    PIN(csum);
    MARK(0, 2);
    {
        float mean, ra;
        ln_scalars(xhalf_sum(xs0 + xs1), xhalf_sum(xq0 + xq1), mean, ra);
        if (!ISV && ct < 5) ra *= QSCALE;
        if (h == 0) { sa[tb0 + r] = ra; sb[tb0 + r] = -mean * ra; }
        if (ISV) { if (h == 0) cst[cb0 + r] = csum[0]; }
        else if (r == 0) {
#pragma unroll
            for (int gg = 0; gg < 4; ++gg) *(LAS f32x4*)(cst + cb0 + 8 * gg + 4 * h) = f32x4{csum[4 * gg], csum[4 * gg + 1], csum[4 * gg + 2], csum[4 * gg + 3]};
        }
    }
    lds_bar();
    const rsrc_t rw = mk_rsrc(p.ws, (unsigned)WS_END);
    if (ISV) {
#pragma unroll
        for (int a = 0; a < 2; ++a) {
            const int tb = a ? tb1 : tb0, T = rb * 4 + (tb >> 5);
            f32x4 av[4], bv[4];
#pragma unroll
            for (int gg = 0; gg < 4; ++gg) { av[gg] = *(const LAS f32x4*)(sa + tb + 8 * gg + 4 * h); bv[gg] = *(const LAS f32x4*)(sb + tb + 8 * gg + 4 * h); }
#pragma unroll
            for (int j = 0; j < 2; ++j) {
                const int cl = (j ? cb1 : cb0) + r;
                const float cs = cst[cl];
                const int c = col0 + cl - 2 * DM, head = c / 20, d = c - head * 20;
                const unsigned dst = (unsigned)WS_KV + (head * 64 + T) * 3072 + 1536 + h * 384 + d * 16;
#pragma unroll
                for (int s = 0; s < 2; ++s) {
                    u32x4 v;
#pragma unroll
                    for (int i = 0; i < 4; ++i) {
                        const int e = 8 * s + 2 * i;
                        v[i] = cvtpk(av[e >> 2][e & 3] * acc[a][j][e] + bv[e >> 2][e & 3] * cs, av[e >> 2][(e & 3) + 1] * acc[a][j][e + 1] + bv[e >> 2][(e & 3) + 1] * cs);
                    }
                    st128(rw, dst + s * 768, v);
                    if (d >= 16) {
                        const unsigned c1 = d == 16 ? 0x3f803f80u : 0u;
                        st128(rw, dst + s * 768 + 64, u32x4{c1, c1, c1, c1});
                    }
                }
            }
        }
    } else {
        const bool isQ = ct < 5;
        const float ra0 = sa[tb0 + r], rb0 = sb[tb0 + r], ra1 = sa[tb1 + r], rb1 = sb[tb1 + r];
#pragma unroll
        for (int j = 0; j < 2; ++j)
#pragma unroll
            for (int gg = 0; gg < 4; ++gg) {
                const int cl = (j ? cb1 : cb0) + 8 * gg + 4 * h;
                const f32x4 cs = *(const LAS f32x4*)(cst + cl);
                const int c4 = (col0 + cl - (isQ ? 0 : DM)) >> 2, head = c4 / 5, grp = c4 - head * 5;
                const unsigned in0 = ((grp >> 1) * 32 + r) * 16 + (grp & 1) * 8;
#pragma unroll
                for (int a = 0; a < 2; ++a) {
                    const float ra = a ? ra1 : ra0, rbb = a ? rb1 : rb0;
                    const int T = rb * 4 + ((a ? tb1 : tb0) >> 5);
                    u32x2 v;
                    v[0] = cvtpk(ra * acc[a][j][4 * gg] + rbb * cs[0], ra * acc[a][j][4 * gg + 1] + rbb * cs[1]);
                    v[1] = cvtpk(ra * acc[a][j][4 * gg + 2] + rbb * cs[2], ra * acc[a][j][4 * gg + 3] + rbb * cs[3]);
                    if (isQ) {
                        st64(rw, grp < 4 ? (unsigned)WS_Q0 + (head * 64 + T) * 1024 + in0 : (unsigned)WS_Q1 + (head * SEQ + T * 32 + r) * 8, v);
                    } else {
                        const unsigned base = (unsigned)WS_KV + (head * 64 + T) * 3072;
                        st64(rw, base + (grp < 4 ? in0 : 1024 + r * 8), v);
                        if (grp == 4) st64(rw, base + 1280 + r * 8, u32x2{0x3f803f80u, 0x00003f80u});
                    }
                }
            }
    }
}
DI void phaseA(const Params& p, LAS char* sm, int b PROF_ARG) {
    const int g = b & 7, i = b >> 3, rb = 4 * (g & 3) + (i & 3), ct = (g >> 2) * 8 + (i >> 2);
    {
        const int l = threadIdx.x & 63, w = uni(threadIdx.x >> 6);
        if (w < 4) {
            const unsigned scr = (unsigned)(uintptr_t)sm + A_STAT + 2048 + w * 256;
            touch(scr, b * 10240 + w * 2560 + l * 64, mk_rsrc(p.x, SEQ * DM * 2));
            touch(scr, b * 9600 + w * 2400 + l * 64, mk_rsrc(p.wqkv, DM * 3 * DM * 2));
        }
        if (w == 3) {
            const unsigned scr = (unsigned)(uintptr_t)sm + A_STAT + 2048 + w * 256;
            const unsigned o = (l * 32 + i) * X_TLB;
            touch(scr, o, mk_rsrc(p.wo, DM * DM * 2));
            touch(scr, o, mk_rsrc(p.w1, SEQ * FF * 2));
            touch(scr, o, mk_rsrc(p.w2, SEQ * FF * 2));
            touch(scr, o, mk_rsrc(p.ws, (unsigned)WS_END));
            touch(scr, o, mk_rsrc(p.out, SEQ * DM * 2));
            touch(scr, o, mk_rsrc(p.x, SEQ * DM * 2));
            touch(scr, o, mk_rsrc(p.wqkv, DM * 3 * DM * 2));
        }
    }
    if (ct >= 15) { asm volatile("s_waitcnt vmcnt(0)" ::: "memory"); return; }
    if (ct >= 10) phaseA_body<true>(p, sm, rb, ct PROF_PASS); else phaseA_body<false>(p, sm, rb, ct PROF_PASS);
    MARK(0, 3);
}

constexpr int B_STAGE = 8 * 3072, B_NS = 3, B_NT = 8, B_FLAG = B_STAGE * B_NS;
constexpr int B_LDS = B_FLAG + 64 + 8 * 1024;

DI u32x2 neg_split3(float m) {
    const unsigned hi = cvtpk(m, 0.f) & 0xffffu; const float r1 = m - bf_lo(hi);
    const unsigned mid = cvtpk(r1, 0.f) & 0xffffu; const float r2 = r1 - bf_lo(mid);
    const unsigned lo = cvtpk(r2, 0.f) & 0xffffu;
    return u32x2{(hi | (mid << 16)) ^ 0x80008000u, lo ^ 0x8000u};
}
DI float max16(const f32x16& s) {
    float a = __builtin_fmaxf(__builtin_fmaxf(s[0], s[1]), s[2]), b = __builtin_fmaxf(__builtin_fmaxf(s[3], s[4]), s[5]);
    a = __builtin_fmaxf(__builtin_fmaxf(a, s[6]), s[7]); b = __builtin_fmaxf(__builtin_fmaxf(b, s[8]), s[9]);
    a = __builtin_fmaxf(__builtin_fmaxf(a, s[10]), s[11]); b = __builtin_fmaxf(__builtin_fmaxf(b, s[12]), s[13]);
    a = __builtin_fmaxf(__builtin_fmaxf(a, s[14]), s[15]);
    return __builtin_fmaxf(a, b);
}
DI bf16x4 ld64(const LAS char* p) { return *(const LAS bf16x4*)p; }

DI void phaseB(const Params& p, LAS char* sm, int b PROF_ARG) {
    const int tid = threadIdx.x, l = tid & 63, w = uni(tid >> 6), r = l & 31, h = l >> 5;
    const int g = b & 7, i = b >> 3, head = 4 * g + (i >> 3);
    const int kh = w >> 2, TA = (i & 7) * 8 + (w & 3) * 2;
    const unsigned sm0 = (unsigned)(uintptr_t)sm;
    const rsrc_t rs = mk_rsrc(p.ws + WS_KV + (size_t)head * 64 * 3072, 64 * 3072);
    const unsigned voff = (kh * 32 + (w & 3)) * 3072 + l * 16;
    auto issue = [&](int t, int slot) {
#pragma unroll
        for (int j = 0; j < 3; ++j) dma16(sm0 + slot * B_STAGE + w * 3072 + j * 1024, voff, rs, t * 4 * 3072 + j * 1024);
    };
    const rsrc_t rpf = mk_rsrc(w < 3 ? p.w1 : w < 6 ? p.w2 : p.wo, w < 6 ? SEQ * FF * 2 : DM * DM * 2);
    const unsigned pfo = (w < 6 ? b * 24576 + (w % 3) * 8192 : b * 3200) + l * 16, pfl = sm0 + B_FLAG + 64 + w * 1024;
    MARK(1, 0);
    issue(0, 0); issue(1, 1);
    const char* qp0 = p.ws + WS_Q0 + ((size_t)(head * 64 + TA) * 64 + l) * 16;
    const char* qp1 = p.ws + WS_Q1 + ((size_t)head * SEQ + TA * 32 + r) * 8;
    bf16x8 qA0 = *(const bf16x8*)qp0, qB0 = *(const bf16x8*)(qp0 + 1024);
    u32x2 qA1u = *(const u32x2*)qp1, qB1u = *(const u32x2*)(qp1 + 256);
    PIN(qA0); PIN(qB0); PIN(qA1u); PIN(qB1u);
    if (h) { qA1u = u32x2{0u, 0u}; qB1u = u32x2{0u, 0u}; }
    const unsigned k0o = l * 16, k1o = 1024 + l * 8, vo = 1536 + h * 384 + (r < 22 ? r : 22) * 16;
    const LAS char* sw = sm + kh * 4 * 3072;
    f32x16 oA, oB;
    float lsum, mA, mB;
    auto scores = [&](const LAS char* tp, bf16x8 q0, u32x2 q1) {
        return mfma32k8(ld64(tp + k1o), __builtin_bit_cast(bf16x4, q1), mfma32(ld128(tp + k0o), q0, f32x16{}));
    };
    for (int pass = 0;; ++pass) {
        if (pass == 1) {
            if (h) { qA1u = u32x2{0u, 0u}; qB1u = u32x2{0u, 0u}; }
            mA = -INFINITY; mB = -INFINITY;
            issue(0, 0); issue(1, 1);
            for (int t = 0; t < B_NT; ++t) {
                if (t + 1 < B_NT) wait_bar<3>(); else wait_bar<0>();
                if (t + 2 < B_NT) issue(t + 2, (t + 2) % B_NS);
                const LAS char* st = sm + (t % B_NS) * B_STAGE;
#pragma unroll 1
                for (int kt = 0; kt < 8; ++kt) {
                    mA = __builtin_fmaxf(mA, max16(scores(st + kt * 3072, qA0, qA1u)));
                    mB = __builtin_fmaxf(mB, max16(scores(st + kt * 3072, qB0, qB1u)));
                }
            }
            mA = xhalf_max(mA); mB = xhalf_max(mB);
            lds_bar();
            if (h) { qA1u = neg_split3(mA); qB1u = neg_split3(mB); }
            issue(0, 0); issue(1, 1);
        }
        wait_bar<3>();
        dma16(pfl, pfo, rpf, 0);
        if (pass == 0) {
            const f32x16 s0 = scores(sw, qA0, qA1u), s1 = scores(sw, qB0, qB1u);
            mA = xhalf_max(max16(s0)); mB = xhalf_max(max16(s1));
            if (h) { qA1u = neg_split3(mA); qB1u = neg_split3(mB); }
        }
        MARK(1, 1);
        const bf16x4 qA1 = __builtin_bit_cast(bf16x4, qA1u), qB1 = __builtin_bit_cast(bf16x4, qB1u);
        oA = f32x16{}; oB = f32x16{};
        bf16x8 kf0 = ld128(sw + k0o), vfa = ld128(sw + vo), vfb, vf1 = bf16x8{};
        bf16x4 kf1 = ld64(sw + k1o);
        f32x16 sA = mfma32k8(kf1, qA1, mfma32(kf0, qA0, f32x16{})), sB;
        u32x4 p0, p1 = u32x4{0u, 0u, 0u, 0u};
#define EX(v) __builtin_amdgcn_exp2f(v)
#define BF(P) __builtin_bit_cast(bf16x8, P)
#define GRP(S, a, P, c) do { \
            const float e0_ = EX(S[a]), e1_ = EX(S[a + 1]), e2_ = EX(S[a + 2]), e3_ = EX(S[a + 3]); \
            P[c] = cvtpk(e0_, e1_); P[c + 1] = cvtpk(e2_, e3_); PIN(P); SBAR(); } while (0)
#define MF(stmt) do { stmt; SBAR(); } while (0)
#define TILE(TC, TN, V0, V0N) do { \
            MF(sB = mfma32(kf0, qB0, f32x16{}));          GRP(sA, 0, p0, 0); \
            MF(oB = mfma32(vf1, BF(p1), oB)); \
            MF((kf0 = ld128((TN) + k0o), vf1 = ld128((TC) + vo + 768), V0N = ld128((TN) + vo))); \
                                                          GRP(sA, 4, p0, 2); \
            MF(sB = mfma32k8(kf1, qB1, sB)); \
            MF(kf1 = ld64((TN) + k1o));                   GRP(sA, 8, p1, 0); \
            MF(oA = mfma32(V0, BF(p0), oA));              GRP(sA, 12, p1, 2); \
            asm volatile("" : "+v"(kf0), "+v"(kf1), "+v"(vf1), "+v"(V0N)); SBAR(); \
            MF(sA = mfma32(kf0, qA0, f32x16{}));          GRP(sB, 0, p0, 0); \
            MF(oA = mfma32(vf1, BF(p1), oA));             GRP(sB, 4, p0, 2); \
            MF(sA = mfma32k8(kf1, qA1, sA));              GRP(sB, 8, p1, 0); \
            MF(oB = mfma32(V0, BF(p0), oB));              GRP(sB, 12, p1, 2); } while (0)
#pragma unroll
        for (int t = 0; t < B_NT; ++t) {
            const LAS char* st = sw + (t % B_NS) * B_STAGE;
            const LAS char* sn = sw + ((t + 1) % B_NS) * B_STAGE;
            TILE(st, st + 1 * 3072, vfa, vfb);
            TILE(st + 1 * 3072, st + 2 * 3072, vfb, vfa);
            TILE(st + 2 * 3072, st + 3 * 3072, vfa, vfb);
            if (t + 1 < B_NT) {
                wait_bar<1>();
                if (t + 2 < B_NT) issue(t + 2, (t + 2) % B_NS);
                dma16(pfl, pfo, rpf, (t + 1) * 1024);
            } else wait_bar<0>();
            TILE(st + 3 * 3072, sn, vfb, vfa);
        }
        oB = mfma32(vf1, BF(p1), oB);
#undef TILE
#undef MF
#undef GRP
#undef BF
#undef EX
        MARK(1, 2);
        lds_bar();
        LAS f32x4* red = (LAS f32x4*)sm + (w & 3) * 7 * 64 + l;
        LAS float* mex = (LAS float*)((LAS f32x4*)sm + ((w & 3) * 7 + 6) * 64);
        f32x16 o = kh ? oB : oA;
        {
            const f32x16 give = kh ? oA : oB;
#pragma unroll
            for (int e = 0; e < 3; ++e) red[(kh * 3 + e) * 64] = f32x4{give[4 * e], give[4 * e + 1], give[4 * e + 2], give[4 * e + 3]};
            mex[kh * 64 + l] = kh ? mA : mB;
        }
        lds_bar();
        {
            const float mo = mex[(kh ^ 1) * 64 + l], mm = kh ? mB : mA, mx = __builtin_fmaxf(mo, mm);
            const float fo = __builtin_amdgcn_exp2f(mo - mx), fm = __builtin_amdgcn_exp2f(mm - mx);
#pragma unroll
            for (int e = 0; e < 3; ++e) {
                const f32x4 v = red[((kh ^ 1) * 3 + e) * 64];
#pragma unroll
                for (int k = 0; k < 4; ++k) o[4 * e + k] = o[4 * e + k] * fm + v[k] * fo;
            }
        }
        oA = o;
        {
            auto rr = __builtin_amdgcn_permlane32_swap(__float_as_uint(o[8]), __float_as_uint(o[8]), false, false);
            lsum = __uint_as_float(rr[1]);
        }
        if (pass == 1) break;
        bool ok = lsum > 0x1p-30f && lsum < 0x1p64f;
#pragma unroll
        for (int e = 0; e < 12; ++e) ok = ok && (e >= 8 && h ? true : __builtin_fabsf(o[e]) < INFINITY);
        LAS int* flag = (LAS int*)(sm + B_FLAG);
        const bool wave_ok = __all(ok);
        if (l == 0) flag[w] = wave_ok ? 0 : 1;
        lds_bar();
        const u32x4 f0 = *(LAS u32x4*)flag, f1 = *(LAS u32x4*)(flag + 4);
        if (!uni(f0[0] | f0[1] | f0[2] | f0[3] | f1[0] | f1[1] | f1[2] | f1[3])) break;
        lds_bar();
    }
    const float inv = __builtin_amdgcn_rcpf(lsum);
    const rsrc_t rw = mk_rsrc(p.ws, (unsigned)WS_END);
    const unsigned orow = (unsigned)WS_O + ((TA + kh) * 32 + r) * (DM * 2);
#pragma unroll
    for (int e = 0; e < 3; ++e) {
        if (e < 2 || h == 0) {
            const int gam = head * 5 + 2 * e + h;
            const int G = gam & 7, pos = (gam & ~7) + 2 * (G & 3) + (G >> 2);
            u32x2 v;
            v[0] = cvtpk(oA[4 * e] * inv, oA[4 * e + 1] * inv); v[1] = cvtpk(oA[4 * e + 2] * inv, oA[4 * e + 3] * inv);
            st64(rw, orow + pos * 8, v);
        }
    }
    MARK(1, 3);
}

constexpr int C_STAGE = 36864, C_NT = 5, C_RED = C_STAGE * 4;
constexpr int C_LDS = C_RED + 8 * 64 * 8 + 64;

DI void phaseC(const Params& p, LAS char* sm, int b PROF_ARG) {
    const int tid = threadIdx.x, l = tid & 63, w = uni(tid >> 6);
    const int gx = b & 7, ix = b >> 3, rb = 4 * gx + (ix >> 3), cb = ix & 7;
    const int row0 = rb * 64, col0 = cb * 80;
    const unsigned sm0 = (unsigned)(uintptr_t)sm;
    const int tk = l & 15, g = l >> 4, q = (l & 15) >> 2, p4 = l & 3;
    const int row = row0 + 16 * (w & 3) + tk;
    const rsrc_t rw = mk_rsrc(p.ws, (unsigned)WS_END);
    float hv[20];
    unsigned long long* gran = (unsigned long long*)(p.ws + WS_ST) + (size_t)rb * 8 * 64 * 2;
    MARK(2, 0);
    if (w >= 4) {
        const int lw = w - 4;
        const rsrc_t rsO = mk_rsrc(p.ws + WS_O, SEQ * DM * 2), rsW = mk_rsrc(p.wo, DM * DM * 2);
        unsigned vo[4], vw[5];
#pragma unroll
        for (int j = 0; j < 4; ++j) { const int rr = 4 * (lw * 4 + j) + (l >> 4), c = (l & 15) ^ (rr & 15); vo[j] = (row0 + rr) * (DM * 2) + c * 16; }
#pragma unroll
        for (int j = 0; j < 5; ++j) { const int ch = (lw * 5 + j) * 64 + l, kr = ch / 10, c = ch - kr * 10; vw[j] = kr * (DM * 2) + col0 * 2 + c * 16; }
        ring_loader<9, C_NT>([&](int t) {
            const unsigned base = sm0 + (t & 3) * C_STAGE;
#pragma unroll
            for (int j = 0; j < 4; ++j) dma16(base + (lw * 4 + j) * 1024, vo[j], rsO, t * 256);
#pragma unroll
            for (int j = 0; j < 5; ++j) dma16(base + 16384 + (lw * 5 + j) * 1024, vw[j], rsW, t * 128 * (DM * 2));
        });
    } else {
        u32x2 xv[5];
        {
            const rsrc_t rx = mk_rsrc(p.x, SEQ * DM * 2);
#pragma unroll
            for (int j = 0; j < 5; ++j) xv[j] = ld64g(rx, (row * DM + col0 + 16 * j + 4 * g) * 2);
        }
        unsigned oa[4];
#pragma unroll
        for (int ks = 0; ks < 4; ++ks) oa[ks] = (16 * w + tk) * 256 + (((4 * ks + g) ^ tk) << 4);
        const unsigned wa = 16384 + (4 * g + q) * 160 + p4 * 8;
        f32x4 acc[5] = {};
        struct Frag { bf16x8 o, w[5]; } F0, F1;
        auto rd = [&](Frag& F, const LAS char* st, int ks) {
            F.o = ld128(st + oa[ks]);
#pragma unroll
            for (int j = 0; j < 5; ++j) F.w[j] = ldtr2(st + wa + ks * 32 * 160 + j * 32, st + wa + ks * 32 * 160 + j * 32 + 16 * 160);
            SBAR();
        };
        auto mm = [&](const Frag& F) {
#pragma unroll
            for (int j = 0; j < 5; ++j) acc[j] = mfma16(F.w[j], F.o, acc[j]);
            SBAR();
        };
        lds_bar();
        MARK(2, 1);
        rd(F0, sm, 0);
#pragma unroll
        for (int t = 0; t < C_NT; ++t) {
            const LAS char* st = sm + (t & 3) * C_STAGE;
            rd(F1, st, 1); mm(F0);
            rd(F0, st, 2); mm(F1);
            rd(F1, st, 3); mm(F0);
            if (t + 1 < C_NT) { lds_bar(); rd(F0, sm + ((t + 1) & 3) * C_STAGE, 0); }
            mm(F1);
        }
        MARK(2, 2);
        float s = 0.f, ss = 0.f;
        u32x2 hb[5];
#pragma unroll
        for (int j = 0; j < 5; ++j) {
            const f32x4 a = acc[j];
            hb[j][0] = cvtpk(a[0] + bf_lo(xv[j][0]), a[1] + bf_hi(xv[j][0])); hb[j][1] = cvtpk(a[2] + bf_lo(xv[j][1]), a[3] + bf_hi(xv[j][1]));
            hv[4 * j] = bf_lo(hb[j][0]); hv[4 * j + 1] = bf_hi(hb[j][0]); hv[4 * j + 2] = bf_lo(hb[j][1]); hv[4 * j + 3] = bf_hi(hb[j][1]);
#pragma unroll
            for (int e = 0; e < 4; ++e) { s += hv[4 * j + e]; ss += hv[4 * j + e] * hv[4 * j + e]; }
        }
        s += __shfl_xor(s, 16); ss += __shfl_xor(ss, 16); s = xhalf_sum(s); ss = xhalf_sum(ss);
        if (g < 2) {
            const unsigned long long v = (1ull << 32) | __float_as_uint(g == 0 ? s : ss);
            __hip_atomic_store(gran + ((size_t)cb * 64 + 16 * w + tk) * 2 + g, v, __ATOMIC_RELAXED, __HIP_MEMORY_SCOPE_AGENT);
        }
#pragma unroll
        for (int j = 0; j < 5; ++j) st64(rw, (unsigned)WS_H + (row * DM + col0 + 16 * j + 4 * g) * 2, hb[j]);
    }
    MARK(2, 3);
    LAS float* fin = (LAS float*)(sm + C_RED);
    {
        unsigned long long a, c; unsigned spins = 0;
        for (;;) {
            a = __hip_atomic_load(gran + ((size_t)w * 64 + l) * 2, __ATOMIC_RELAXED, __HIP_MEMORY_SCOPE_AGENT);
            c = __hip_atomic_load(gran + ((size_t)w * 64 + l) * 2 + 1, __ATOMIC_RELAXED, __HIP_MEMORY_SCOPE_AGENT);
            if (__all((a >> 32) == 1ull && (c >> 32) == 1ull)) break;
            if (++spins > (1u << 20)) break;
            __builtin_amdgcn_s_sleep(1);
        }
        *(LAS u32x2*)(fin + (w * 64 + l) * 2) = u32x2{(unsigned)a, (unsigned)c};
    }
    lds_bar();
    MARK(2, 4);
    if (w < 4) {
        float s = 0.f, ss = 0.f;
#pragma unroll
        for (int j = 0; j < 8; ++j) { const u32x2 v = *(const LAS u32x2*)(fin + (j * 64 + 16 * w + tk) * 2); s += __uint_as_float(v[0]); ss += __uint_as_float(v[1]); }
        const float m = s * (1.f / DM), var = fmaxf(ss * (1.f / DM) - m * m, 0.f);
        const float r1 = __builtin_amdgcn_rsqf(var + EPS);
        const float r2 = __builtin_amdgcn_rsqf(var * r1 * r1 + EPS), rr = r1 * r2;
#pragma unroll
        for (int j = 0; j < 5; ++j) {
            u32x2 v;
            v[0] = cvtpk((hv[4 * j] - m) * rr, (hv[4 * j + 1] - m) * rr); v[1] = cvtpk((hv[4 * j + 2] - m) * rr, (hv[4 * j + 3] - m) * rr);
            st64(rw, (unsigned)WS_HN + (row * DM + col0 + 16 * j + 4 * g) * 2, v);
        }
    }
    MARK(2, 5);
}

constexpr int D_STAGE = 32768, D_NT = 16;
constexpr int D_LDS = D_STAGE * 4;

DI float gelu_tanh(float v) {
    const float y = v * (0.7978845608028654f + 0.7978845608028654f * 0.044715f * v * v);
    return v * __builtin_amdgcn_rcpf(1.f + __builtin_amdgcn_exp2f(-2.f * 1.4426950408889634f * y));
}
DI void phaseD(const Params& p, LAS char* sm, int b PROF_ARG) {
    const int gx = b & 7, ix = b >> 3;
    if (ix >= 30) return;
    const int db = 5 * (gx & 1) + ix % 5, fb = 6 * (gx >> 1) + ix / 5;
    const int tid = threadIdx.x, l = tid & 63, w = uni(tid >> 6);
    const unsigned sm0 = (unsigned)(uintptr_t)sm;
    MARK(3, 0);
    if (w >= 4) {
        const bool isH = w < 6;
        const int pitch = isH ? DM * 2 : FF * 2;
        const rsrc_t rs = mk_rsrc(isH ? (const void*)(p.ws + WS_HN) : (const void*)p.w1, isH ? SEQ * DM * 2 : SEQ * FF * 2);
        const int kr = 64 * (w & 1) + (l >> 3);
        const unsigned voff = kr * pitch + (isH ? db : fb) * 128 + (((l & 7) ^ (((kr >> 1) & 1) << 2)) << 4);
        const unsigned ldst = sm0 + (w - 4) * 8192;
        ring_loader<8, D_NT>([&](int t) {
#pragma unroll
            for (int j = 0; j < 8; ++j) dma16(ldst + (t & 3) * D_STAGE + j * 1024, voff, rs, (unsigned)((t * 128 + j * 8) * pitch));
        });
        return;
    }
    const int wf = w >> 1, wd = w & 1, r = l & 31, h = l >> 5;
    const int g = l >> 4, q = (l & 15) >> 2, p4 = l & 3;
    u32x2 bv[4];
    {
        const rsrc_t rb1 = mk_rsrc(p.b1, FF * 2);
#pragma unroll
        for (int e = 0; e < 4; ++e) bv[e] = ld64g(rb1, (fb * 64 + 32 * wf + 8 * e + 4 * h) * 2);
    }
    const unsigned rowo = (8 * (g >> 1) + q) * 128 + (p4 & 1) * 8, sw = ((q >> 1) & 1) << 2;
    const unsigned ha = rowo + (((4 * wd + 2 * (g & 1) + (p4 >> 1)) ^ sw) << 4);
    const unsigned wa = 16384 + rowo + (((4 * wf + 2 * (g & 1) + (p4 >> 1)) ^ sw) << 4);
    f32x16 acc = {};
    struct Frag { bf16x8 h, w; } F0, F1;
    auto rd = [&](Frag& F, const LAS char* st, int ks) {
        F.h = ldtr2(st + ha + ks * 2048, st + ha + ks * 2048 + 512);
        F.w = ldtr2(st + wa + ks * 2048, st + wa + ks * 2048 + 512);
        SBAR();
    };
    auto mm = [&](const Frag& F) { acc = mfma32(F.w, F.h, acc); SBAR(); };
    lds_bar();
    MARK(3, 1);
    rd(F0, sm, 0);
#pragma unroll
    for (int t = 0; t < D_NT; ++t) {
        const LAS char* st = sm + (t & 3) * D_STAGE;
        rd(F1, st, 1); mm(F0);
        rd(F0, st, 2); mm(F1);
        rd(F1, st, 3); mm(F0);
        rd(F0, st, 4); mm(F1);
        rd(F1, st, 5); mm(F0);
        rd(F0, st, 6); mm(F1);
        rd(F1, st, 7); mm(F0);
        if (t + 1 < D_NT) { lds_bar(); rd(F0, sm + ((t + 1) & 3) * D_STAGE, 0); }
        mm(F1);
    }
    MARK(3, 2);
    {
        const rsrc_t rw = mk_rsrc(p.ws, (unsigned)WS_END);
        const int d = db * 64 + 32 * wd + r;
#pragma unroll
        for (int e = 0; e < 4; ++e) {
            const int f0 = fb * 64 + 32 * wf + 8 * e + 4 * h;
            u32x2 v;
            v[0] = cvtpk(gelu_tanh(acc[4 * e] + bf_lo(bv[e][0])), gelu_tanh(acc[4 * e + 1] + bf_hi(bv[e][0])));
            v[1] = cvtpk(gelu_tanh(acc[4 * e + 2] + bf_lo(bv[e][1])), gelu_tanh(acc[4 * e + 3] + bf_hi(bv[e][1])));
            st64(rw, (unsigned)WS_U + (d * FF + f0) * 2, v);
        }
    }
    MARK(3, 3);
}

constexpr int E_STAGE = 36864, E_NT = 12;
constexpr int E_LDS = E_STAGE * 4;

DI void phaseE(const Params& p, LAS char* sm, int b PROF_ARG) {
    const int gx = b & 7, ix = b >> 3, db = 4 * (gx & 1) + (ix & 3), sb = 8 * (gx >> 1) + (ix >> 2);
    const int d0 = db * 80, s0 = sb * 64;
    const int tid = threadIdx.x, l = tid & 63, w = uni(tid >> 6);
    const unsigned sm0 = (unsigned)(uintptr_t)sm;
    MARK(4, 0);
    if (w >= 4) {
        const int lw = w - 4;
        const rsrc_t rsU = mk_rsrc(p.ws + WS_U, DM * FF * 2), rsW = mk_rsrc(p.w2, FF * SEQ * 2);
        unsigned vu[5], vw[2];
#pragma unroll
        for (int j = 0; j < 5; ++j) { const int rr = 4 * (lw * 5 + j) + (l >> 4), c = (l & 15) ^ (rr & 15); vu[j] = (d0 + rr) * (FF * 2) + c * 16; }
#pragma unroll
        for (int j = 0; j < 2; ++j) {
            const int kr = 8 * (lw * 4 + j) + (l >> 3), fr = ((kr >> 1) & 1) | (((kr >> 3) & 1) << 1);
            vw[j] = kr * (SEQ * 2) + s0 * 2 + (((l & 7) ^ (fr << 1)) << 4);
        }
        ring_loader<9, E_NT>([&](int t) {
            const unsigned base = sm0 + (t & 3) * E_STAGE;
#pragma unroll
            for (int j = 0; j < 5; ++j) dma16(base + (lw * 5 + j) * 1024, vu[j], rsU, t * 256);
#pragma unroll
            for (int j = 0; j < 4; ++j) dma16(base + 20480 + (lw * 4 + j) * 1024, vw[j & 1], rsW, (unsigned)((t * 128 + (j >> 1) * 16) * (SEQ * 2)));
        });
        return;
    }
    const int tk = l & 15, g = l >> 4, q = (l & 15) >> 2, p4 = l & 3;
    const int srow = s0 + 16 * w + tk;
    u32x2 hv[5];
    unsigned b2u;
    {
        const rsrc_t rh = mk_rsrc(p.ws + WS_H, SEQ * DM * 2), rb2 = mk_rsrc(p.b2, SEQ * 2);
#pragma unroll
        for (int j = 0; j < 5; ++j) hv[j] = ld64g(rh, (srow * DM + d0 + 16 * j + 4 * g) * 2);
        b2u = __builtin_amdgcn_raw_buffer_load_b16(rb2, srow * 2, 0, 0);
    }
    const int fr = ((q >> 1) & 1) | ((g & 1) << 1);
    const unsigned wa = 20480 + (8 * g + q) * 128 + (((2 * w + (p4 >> 1)) ^ (fr << 1)) << 4) + (p4 & 1) * 8;
    unsigned uo[4];
#pragma unroll
    for (int ks = 0; ks < 4; ++ks) uo[ks] = tk * 256 + (((4 * ks + g) ^ tk) << 4);
    f32x4 acc[5] = {};
    struct Frag { bf16x8 w, u[5]; } F0, F1;
    auto rd = [&](Frag& F, const LAS char* st, int ks) {
        F.w = ldtr2(st + wa + ks * 4096, st + wa + ks * 4096 + 512);
#pragma unroll
        for (int j = 0; j < 5; ++j) F.u[j] = ld128(st + uo[ks] + j * 4096);
        SBAR();
    };
    auto mm = [&](const Frag& F) {
#pragma unroll
        for (int j = 0; j < 5; ++j) acc[j] = mfma16(F.u[j], F.w, acc[j]);
        SBAR();
    };
    lds_bar();
    MARK(4, 1);
    rd(F0, sm, 0);
#pragma unroll
    for (int t = 0; t < E_NT; ++t) {
        const LAS char* st = sm + (t & 3) * E_STAGE;
        rd(F1, st, 1); mm(F0);
        rd(F0, st, 2); mm(F1);
        rd(F1, st, 3); mm(F0);
        if (t + 1 < E_NT) { lds_bar(); rd(F0, sm + ((t + 1) & 3) * E_STAGE, 0); }
        mm(F1);
    }
    MARK(4, 2);
    {
        const rsrc_t ro = mk_rsrc(p.out, SEQ * DM * 2);
        const float bias = bf_lo(b2u);
#pragma unroll
        for (int j = 0; j < 5; ++j) {
            const f32x4 a = acc[j];
            u32x2 v;
            v[0] = cvtpk(bf_lo(hv[j][0]) + (a[0] + bias), bf_hi(hv[j][0]) + (a[1] + bias));
            v[1] = cvtpk(bf_lo(hv[j][1]) + (a[2] + bias), bf_hi(hv[j][1]) + (a[3] + bias));
            __builtin_amdgcn_raw_buffer_store_b64(v, ro, (srow * DM + d0 + 16 * j + 4 * g) * 2, 0, 0);
        }
    }
    MARK(4, 3);
}

__global__ void __launch_bounds__(512, 2) k_phaseA(Params p) {
    extern __shared__ __attribute__((aligned(16))) char smem[];
    { const int i = blockIdx.x * 512 + threadIdx.x; if (i < (int)(WS_ST_BYTES / 16)) *(u32x4*)(p.ws + WS_ST + (size_t)i * 16) = u32x4{0u, 0u, 0u, 0u}; }
    PROF_DECL phaseA(p, (LAS char*)smem, blockIdx.x PROF_PASS); PROF_END(0);
}
__global__ void __launch_bounds__(512, 2) k_phaseB(Params p) { extern __shared__ __attribute__((aligned(16))) char smem[]; PROF_DECL phaseB(p, (LAS char*)smem, blockIdx.x PROF_PASS); PROF_END(1); }
__global__ void __launch_bounds__(512, 2) k_phaseC(Params p) { extern __shared__ __attribute__((aligned(16))) char smem[]; PROF_DECL phaseC(p, (LAS char*)smem, blockIdx.x PROF_PASS); PROF_END(2); }
__global__ void __launch_bounds__(512, 2) k_phaseD(Params p) { extern __shared__ __attribute__((aligned(16))) char smem[]; PROF_DECL phaseD(p, (LAS char*)smem, blockIdx.x PROF_PASS); PROF_END(3); }
__global__ void __launch_bounds__(512, 2) k_phaseE(Params p) { extern __shared__ __attribute__((aligned(16))) char smem[]; PROF_DECL phaseE(p, (LAS char*)smem, blockIdx.x PROF_PASS); PROF_END(4); }

extern "C" void kernel_launch(void* const* d_in, const int* in_sizes, int n_in, void* d_out, int out_size, void* d_ws, size_t ws_size,
                              hipStream_t stream) {
    Params p;
    p.x = (const uint16_t*)d_in[0]; p.wqkv = (const uint16_t*)d_in[1]; p.wo = (const uint16_t*)d_in[2];
    p.w1 = (const uint16_t*)d_in[4]; p.b1 = (const uint16_t*)d_in[5]; p.w2 = (const uint16_t*)d_in[6]; p.b2 = (const uint16_t*)d_in[7];
    p.out = (uint16_t*)d_out; p.ws = (char*)d_ws;
    (void)hipFuncSetAttribute((const void*)k_phaseA, hipFuncAttributeMaxDynamicSharedMemorySize, A_LDS);
    (void)hipFuncSetAttribute((const void*)k_phaseB, hipFuncAttributeMaxDynamicSharedMemorySize, B_LDS);
    (void)hipFuncSetAttribute((const void*)k_phaseC, hipFuncAttributeMaxDynamicSharedMemorySize, C_LDS);
    (void)hipFuncSetAttribute((const void*)k_phaseD, hipFuncAttributeMaxDynamicSharedMemorySize, D_LDS);
    (void)hipFuncSetAttribute((const void*)k_phaseE, hipFuncAttributeMaxDynamicSharedMemorySize, E_LDS);
    k_phaseA<<<256, 512, A_LDS, stream>>>(p);
    k_phaseB<<<256, 512, B_LDS, stream>>>(p);
    k_phaseC<<<256, 512, C_LDS, stream>>>(p);
    k_phaseD<<<256, 512, D_LDS, stream>>>(p);
    k_phaseE<<<256, 512, E_LDS, stream>>>(p);
}
```

```cpp
#include <hip/hip_runtime.h>
#include <hip/hip_bf16.h>
#include <stdint.h>

#define LAS __attribute__((address_space(3)))
#define GAS __attribute__((address_space(1)))
#define DI __device__ __forceinline__
typedef __attribute__((ext_vector_type(8))) short bf16x8;
typedef __attribute__((ext_vector_type(4))) short bf16x4;
typedef __attribute__((ext_vector_type(2))) __bf16 bf16x2v;
typedef __attribute__((ext_vector_type(16))) float f32x16;
typedef __attribute__((ext_vector_type(4))) float f32x4;
typedef __attribute__((ext_vector_type(4))) unsigned u32x4;
typedef __attribute__((ext_vector_type(2))) unsigned u32x2;
typedef __amdgpu_buffer_rsrc_t rsrc_t;

constexpr int SEQ = 2048, DM = 640, NH = 32, FF = 1536;
constexpr float EPS = 1e-5f;
constexpr float QSCALE = 0.22360679774997896f * 1.4426950408889634f;

constexpr unsigned WS_KV = 0;
constexpr unsigned WS_Q  = WS_KV + NH * 64 * 3072;
constexpr unsigned WS_O  = WS_Q + NH * 64 * 1280;
constexpr unsigned WS_H  = WS_O + SEQ * DM * 2;
constexpr unsigned WS_HN = WS_H + SEQ * DM * 2;
constexpr unsigned WS_U  = WS_HN + SEQ * DM * 2;
constexpr unsigned WS_ST = WS_U + DM * FF * 2;
constexpr unsigned WS_SA = WS_ST + 32 * 8 * 64 * 16;
constexpr unsigned WS_SC = WS_SA + SEQ * 16;
constexpr unsigned WS_EP = WS_SC + 10 * 3 * DM * 8;
constexpr unsigned WS_FA = WS_EP + 256;
constexpr unsigned WS_FB = WS_FA + 32 * 1024;
constexpr unsigned WS_FC = WS_FB + 32 * 1024;
constexpr unsigned WS_FD = WS_FC + 32 * 1024;
constexpr unsigned WS_END = WS_FD + 32 * 1280;

DI rsrc_t mk_rsrc(const void* p, unsigned bytes) { return __builtin_amdgcn_make_buffer_rsrc((void*)p, (short)0, (int)bytes, 0x00020000); }
template <bool SC1> DI void dma16(unsigned lds, unsigned voff, rsrc_t rs, unsigned soff) {
    if (SC1) asm volatile("s_mov_b32 m0, %0\n\ts_nop 0\n\tbuffer_load_dwordx4 %1, %2, %3 offen sc1 lds" ::"s"(lds), "v"(voff), "s"(rs), "s"(soff) : "memory");
    else     asm volatile("s_mov_b32 m0, %0\n\ts_nop 0\n\tbuffer_load_dwordx4 %1, %2, %3 offen lds" ::"s"(lds), "v"(voff), "s"(rs), "s"(soff) : "memory");
}
DI void warm_lds(unsigned lds, unsigned voff, rsrc_t rs) {
    asm volatile("s_mov_b32 m0, %0\n\ts_nop 0\n\tbuffer_load_dword %1, %2, 0 offen lds" ::"s"(lds), "v"(voff), "s"(rs) : "memory");
}
template <int N> DI void wait_bar() { asm volatile("s_waitcnt vmcnt(%0) lgkmcnt(0)\n\ts_barrier" ::"i"(N) : "memory"); }
DI void lds_bar() { asm volatile("s_waitcnt lgkmcnt(0)\n\ts_barrier" ::: "memory"); }
DI unsigned cvtpk(float lo, float hi) {
    typedef __attribute__((ext_vector_type(2))) float f32x2;
    return __builtin_bit_cast(unsigned, __builtin_convertvector(f32x2{lo, hi}, bf16x2v));
}
DI float bf_lo(unsigned u) { return __uint_as_float(u << 16); }
DI float bf_hi(unsigned u) { return __uint_as_float(u & 0xffff0000u); }
DI bf16x4 ldtr(const LAS char* p) { return __builtin_amdgcn_ds_read_tr16_b64_v4i16((LAS bf16x4*)p); }
DI bf16x8 ldtr2(const LAS char* p0, const LAS char* p1) {
    const bf16x4 a = ldtr(p0), b = ldtr(p1);
    return __builtin_shufflevector(a, b, 0, 1, 2, 3, 4, 5, 6, 7);
}
DI bf16x8 ld128(const LAS char* p) { return *(const LAS bf16x8*)p; }
struct Anch { unsigned a[3]; };
DI Anch mk_anch(unsigned base) {
    Anch r{{base, base + 65536u, base + 131072u}};
    asm volatile("" : "+v"(r.a[0]), "+v"(r.a[1]), "+v"(r.a[2]));
    return r;
}
DI const LAS char* at(const Anch& A, int o) { return (const LAS char*)(uintptr_t)A.a[o >> 16] + (o & 65535); }
DI f32x16 mfma32(bf16x8 a, bf16x8 b, f32x16 c) { return __builtin_amdgcn_mfma_f32_32x32x16_bf16(a, b, c, 0, 0, 0); }
DI f32x16 mfma32k8(bf16x4 a, bf16x4 b, f32x16 c) { return __builtin_amdgcn_mfma_f32_32x32x8bf16_1k(a, b, c, 0, 0, 0); }
DI f32x4 mfma16(bf16x8 a, bf16x8 b, f32x4 c) { return __builtin_amdgcn_mfma_f32_16x16x32_bf16(a, b, c, 0, 0, 0); }
DI float xhalf_sum(float v) {
    auto r = __builtin_amdgcn_permlane32_swap(__float_as_uint(v), __float_as_uint(v), false, false);
    return __uint_as_float(r[0]) + __uint_as_float(r[1]);
}
DI float xrow_sum(float v) {
    auto r = __builtin_amdgcn_permlane16_swap(__float_as_uint(v), __float_as_uint(v), false, false);
    return __uint_as_float(r[0]) + __uint_as_float(r[1]);
}
DI float xhalf_max(float v) {
    auto r = __builtin_amdgcn_permlane32_swap(__float_as_uint(v), __float_as_uint(v), false, false);
    return __builtin_fmaxf(__uint_as_float(r[0]), __uint_as_float(r[1]));
}
DI int uni(int v) { return __builtin_amdgcn_readfirstlane(v); }
#define PIN(x) asm volatile("" : "+v"(x))
#define SBAR() __builtin_amdgcn_sched_barrier(0)
DI void st64(rsrc_t rs, unsigned off, u32x2 v) { __builtin_amdgcn_raw_buffer_store_b64(v, rs, off, 0, 16); }
DI void st128(rsrc_t rs, unsigned off, u32x4 v) { __builtin_amdgcn_raw_buffer_store_b128(v, rs, off, 0, 16); }
template <int AUX> DI u32x2 ld64g(rsrc_t rs, unsigned off) { return __builtin_bit_cast(u32x2, __builtin_amdgcn_raw_buffer_load_b64(rs, off, 0, AUX)); }
template <int AUX> DI u32x4 ld128g(rsrc_t rs, unsigned off) { return __builtin_bit_cast(u32x4, __builtin_amdgcn_raw_buffer_load_b128(rs, off, 0, AUX)); }
DI float rbf(float v) { return bf_lo(cvtpk(v, 0.f)); }
DI void ln_scalars(float s, float ss, float& mean, float& rstd) {
    const float m = s * (1.f / DM), var = fmaxf(ss * (1.f / DM) - m * m, 0.f);
    mean = rbf(m);
    rstd = __builtin_amdgcn_rcpf(rbf(__builtin_amdgcn_sqrtf(rbf(rbf(var) + EPS))));
}

constexpr int NREP = 16;
constexpr int W1T = 6;
constexpr int W3T = 3;
constexpr int W2T = 1;
constexpr int OUT_AUX = 0;
constexpr int PW1 = 6;
constexpr int PW2 = 5;
struct Params {
    const uint16_t* x; const uint16_t* wqkv; const uint16_t* wo; const uint16_t* w1; const uint16_t* b1; const uint16_t* w2; const uint16_t* b2;
    uint16_t* out; char* ws;
};

DI unsigned tag_of(unsigned ep) { return ep * 0x9E3779B1u + 0x7F4A7C15u; }
DI void publish(const Params& p, unsigned off, unsigned rep_stride, unsigned tag, int l) {
    if (l < NREP) __hip_atomic_store((GAS unsigned*)(p.ws + off + l * rep_stride), tag, __ATOMIC_RELAXED, __HIP_MEMORY_SCOPE_AGENT);
}
template <int N> DI void poll(const Params& p, const unsigned (&off)[N], unsigned tag) {
    for (unsigned spins = 0; spins < (1u << 16); ++spins) {
        unsigned v[N], diff = 0;
#pragma unroll
        for (int k = 0; k < N; ++k) v[k] = __hip_atomic_load((GAS unsigned*)(p.ws + off[k]), __ATOMIC_RELAXED, __HIP_MEMORY_SCOPE_AGENT);
#pragma unroll
        for (int k = 0; k < N; ++k) diff |= v[k] ^ tag;
        if (__all(diff == 0)) break;
        __builtin_amdgcn_s_sleep(1);
    }
    asm volatile("" ::: "memory");
}

template <int NP> DI void wait_bar_stages(int n) {
    if (n <= 0) wait_bar<0>(); else if (n == 1) wait_bar<NP>(); else if (n == 2) wait_bar<2 * NP>(); else if (n == 3) wait_bar<3 * NP>();
    else if (n == 4) wait_bar<4 * NP>(); else wait_bar<5 * NP>();
}
template <int NP, int NT, int NS, int PRE, int LA, class Issue> DI void ring_loader(Issue issue) {
    wait_bar_stages<NP>(PRE - 1);
#pragma unroll
    for (int t = PRE; t < LA; ++t) issue(t);
#pragma unroll
    for (int t = 0; t + 1 < NT; ++t) {
        const int ahead = t + LA - 1 < NT - 1 ? t + LA - 1 : NT - 1, last = ahead > PRE - 1 ? ahead : PRE - 1;
        wait_bar_stages<NP>(last - (t + 1));
        if (t + LA < NT && t + LA >= PRE) issue(t + LA);
    }
}

constexpr int A_STAGE = 32768, A_NT = 10, A_STAT = A_STAGE * 4;

DI void put_granule(const Params& p, unsigned off, unsigned tag, float v) {
    __hip_atomic_store((GAS unsigned long long*)(p.ws + off), ((unsigned long long)tag << 32) | __float_as_uint(v), __ATOMIC_RELAXED, __HIP_MEMORY_SCOPE_AGENT);
}
struct StatsA {
    u32x4 xv[3];
    DI void request(const Params& p, int rb, int ct, int w, int l) {
        const rsrc_t rx = mk_rsrc(p.x, SEQ * DM * 2);
#pragma unroll
        for (int j = 0; j < 3; ++j) {
            const int c = (l & 31) + 32 * j;
            xv[j] = ld128g<0>(rx, c < 80 ? (rb * 128 + ct * 8 + 2 * w + (l >> 5)) * (DM * 2) + c * 16 : 0xffffff00u);
        }
    }
    DI void tokens(const Params& p, int rb, int ct, int w, int l, unsigned tag) const {
        float s = 0.f, ss = 0.f;
#pragma unroll
        for (int j = 0; j < 3; ++j)
#pragma unroll
            for (int i = 0; i < 4; ++i) { const float a = bf_lo(xv[j][i]), b = bf_hi(xv[j][i]); s += a + b; ss += a * a + b * b; }
#pragma unroll
        for (int m = 1; m < 32; m <<= 1) { s += __shfl_xor(s, m); ss += __shfl_xor(ss, m); }
        float mean, ra;
        ln_scalars(s, ss, mean, ra);
        if ((l & 31) < 2) put_granule(p, WS_SA + (rb * 128 + ct * 8 + 2 * w + (l >> 5)) * 16 + (l & 1) * 8, tag, (l & 1) ? -mean * ra : ra);
    }
};

template <bool ISV>
DI void phaseA_body(const Params& p, LAS char* sm, int rb, int ct, unsigned ep, unsigned& tag) {
    const int tid = threadIdx.x, l = tid & 63, w = uni(tid >> 6), r = l & 31, h = l >> 5;
    const int row0 = rb * 128, col0 = ct * 128;
    const unsigned sm0 = (unsigned)(uintptr_t)sm;
    LAS float* sa = (LAS float*)(sm + A_STAT);
    LAS float* sb = sa + 128;
    LAS float* cst = sa + 256;
    const int t0 = rb < A_NT ? rb : rb - A_NT;
    if (w >= 4) {
        const bool isX = w < 6;
        const rsrc_t rs = mk_rsrc(isX ? (const void*)p.x : (const void*)p.wqkv, isX ? SEQ * DM * 2 : DM * 3 * DM * 2);
        unsigned voff[2];
#pragma unroll
        for (int j = 0; j < 2; ++j) {
            const int u = (w & 1) * 8 + j;
            const int ra = 8 * u + (l >> 3), ca = (l & 7) ^ ((ra >> 1) & 7);
            const int kb = 4 * u + (l >> 4), cb = (l & 15) ^ ((kb & 3) << 2);
            voff[j] = isX ? (unsigned)((row0 + ra) * (DM * 2) + ca * 16) : (unsigned)(kb * (3 * DM * 2) + col0 * 2 + cb * 16);
        }
        const unsigned kstep = isX ? 128u : 64u * (3 * DM * 2), pstep = isX ? 16u * (DM * 2) : 8u * (3 * DM * 2);
        const unsigned ldst = sm0 + (w - 4) * 8192;
        auto issue = [&](int t) {
            const int ts = t + t0 < A_NT ? t + t0 : t + t0 - A_NT;
#pragma unroll
            for (int j = 0; j < 8; ++j) dma16<false>(ldst + (t & 3) * A_STAGE + j * 1024, voff[j & 1], rs, kstep * ts + pstep * (j >> 1));
        };
        issue(0); issue(1);
        const rsrc_t rw = mk_rsrc(p.ws, WS_END);
        if (ct >= 5) {
            const int part = ISV ? 2 * DM : DM;
#pragma unroll
            for (int j = 0; j < 2; ++j) {
                const int head = (col0 - part) / 20 + 4 * j + (l >> 4), last = part + head * 20 + 19;
                const unsigned base = WS_KV + (head * 64 + rb * 4 + (w - 4)) * 3072;
                if (last >= col0 && last < col0 + 128) {
                    if (ISV) {
                        const unsigned c1 = (l & 3) == 0 ? 0x3f803f80u : 0u;
                        st128(rw, base + 1536 + ((l >> 3) & 1) * 768 + ((l >> 2) & 1) * 384 + (20 + (l & 3)) * 16, u32x4{c1, c1, c1, c1});
                    } else
                        st128(rw, base + 1280 + (l & 15) * 16, u32x4{0x3f803f80u, 0x00003f80u, 0x3f803f80u, 0x00003f80u});
                }
            }
        }
        ring_loader<8, A_NT, 4, 2, 4>(issue);
        lds_bar();
        tag = tag_of(ep);
        if (w == 4) publish(p, WS_FA + (ct * 16 + rb) * 4, 1024, tag, l);
        return;
    }
    const int wm = w >> 1, wn = w & 1;
    const int tb0 = 64 * wm, tb1 = tb0 + 32, cb0 = 64 * wn + 32 * wm, cb1 = cb0 ^ 32;
    const int g = l >> 4, q = (l & 15) >> 2, p4 = l & 3;
    unsigned xa[4];
#pragma unroll
    for (int ks = 0; ks < 4; ++ks) xa[ks] = ((tb0 + r) * 128 + ((h ^ ((r >> 1) & 7)) << 4)) ^ (ks << 5);
    const unsigned wa = 16384 + (8 * (g >> 1) + q) * 256 + ((((cb0 >> 3) + 2 * (g & 1) + (p4 >> 1)) ^ (q << 2)) << 4) + (p4 & 1) * 8;
    f32x16 acc[2][2] = {};
    struct Frag { bf16x8 x0, x1, w0, w1; } F0, F1;
    Anch ax0[4];
#pragma unroll
    for (int ks = 0; ks < 4; ++ks) ax0[ks] = mk_anch(sm0 + xa[ks]);
    const Anch aw0 = mk_anch(sm0 + wa), aw1 = mk_anch(sm0 + (wa ^ 64));
    auto mf = [&](bf16x8 x, bf16x8 wv, f32x16 c) { return ISV ? mfma32(x, wv, c) : mfma32(wv, x, c); };
    f32x16 csum = {};
    const bf16x8 ones = __builtin_bit_cast(bf16x8, u32x4{0x3f803f80u, 0x3f803f80u, 0x3f803f80u, 0x3f803f80u});
    auto step = [&](const Frag& C, Frag& N, int t, int ks, bool more, bool sums) {
        const int st = (t & 3) * A_STAGE;
        acc[0][0] = mf(C.x0, C.w0, acc[0][0]); SBAR();
        if (more) N.x0 = ld128(at(ax0[ks], st));
        SBAR();
        acc[1][0] = mf(C.x1, C.w0, acc[1][0]); SBAR();
        if (more) N.w0 = ldtr2(at(aw0, st + ks * 4096), at(aw0, st + ks * 4096 + 1024));
        SBAR();
        if (sums) csum = mfma32(ones, C.w0, csum);
        SBAR();
        if (more) N.x1 = ld128(at(ax0[ks], st + 4096));
        SBAR();
        acc[0][1] = mf(C.x0, C.w1, acc[0][1]); SBAR();
        if (more) N.w1 = ldtr2(at(aw1, st + ks * 4096), at(aw1, st + ks * 4096 + 1024));
        SBAR();
        acc[1][1] = mf(C.x1, C.w1, acc[1][1]); SBAR();
    };
    StatsA sta;
    sta.request(p, rb, ct, w, l);
    const rsrc_t rw = mk_rsrc(p.ws, WS_END);
    const unsigned gidx = WS_SA + (row0 + (tid & 127)) * 16, cidx = WS_SC + ((tid >> 7) * 5 * 3 * DM + col0 + (tid & 127)) * 8;
    unsigned eo[8];
    auto addresses = [&]() {
        if (ISV) {
#pragma unroll
            for (int j = 0; j < 2; ++j) {
                const int c = col0 + (j ? cb1 : cb0) + r - 2 * DM, head = c / 20, d = c - head * 20;
                eo[j] = WS_KV + (head * 64 + rb * 4) * 3072 + 1536 + h * 384 + d * 16;
            }
            asm volatile("" : "+v"(eo[0]), "+v"(eo[1]));
        } else {
            const bool isQ = ct < 5;
            const unsigned ts = isQ ? 1280 : 3072, base = (isQ ? WS_Q : WS_KV) + rb * 4 * ts;
#pragma unroll
            for (int e = 0; e < 8; ++e) {
                const int cl = (e >> 2 ? cb1 : cb0) + 8 * (e & 3) + 4 * h;
                const int c4 = (col0 + cl - (isQ ? 0 : DM)) >> 2, head = c4 / 5, grp = c4 - head * 5;
                eo[e] = base + head * 64 * ts + grp * 256 + r * 8;
            }
            asm volatile("" : "+v"(eo[0]), "+v"(eo[1]), "+v"(eo[2]), "+v"(eo[3]), "+v"(eo[4]), "+v"(eo[5]), "+v"(eo[6]), "+v"(eo[7]));
        }
    };
    u32x4 gv;
    u32x2 cv[5];
    auto gather = [&]() {
        if (w < 2) gv = ld128g<16>(rw, gidx);
#pragma unroll
        for (int j = 0; j < 5; ++j) cv[j] = ld64g<16>(rw, cidx + j * 3 * DM * 8);
    };
    lds_bar();
    F0.x0 = ld128(at(ax0[0], 0)); F0.w0 = ldtr2(at(aw0, 0), at(aw0, 1024)); F0.x1 = ld128(at(ax0[0], 4096)); F0.w1 = ldtr2(at(aw1, 0), at(aw1, 1024));
#pragma unroll
    for (int t = 0; t < A_NT; ++t) {
        step(F0, F1, t, 1, true, t == 0);
        step(F1, F0, t, 2, true, t == 0);
        step(F0, F1, t, 3, true, t == 0);
        if (t == 0) { tag = tag_of(ep); sta.tokens(p, rb, ct, w, l, tag); }
        if (t == A_NT - 2) {
            for (unsigned spins = 0; spins < (1u << 16); ++spins) {
                unsigned diff = (cv[0][1] ^ tag) | (cv[1][1] ^ tag) | (cv[2][1] ^ tag) | (cv[3][1] ^ tag) | (cv[4][1] ^ tag);
                if (w < 2) diff |= (gv[1] ^ tag) | (gv[3] ^ tag);
                if (__all(diff == 0)) break;
                __builtin_amdgcn_s_sleep(1);
                gather();
                asm volatile("" ::: "memory");
            }
            if (w < 2) {
                const float sc = !ISV && ct < 5 ? QSCALE : 1.f;
                sa[tid] = __uint_as_float(gv[0]) * sc; sb[tid] = __uint_as_float(gv[2]) * sc;
            }
            cst[tid] = ((__uint_as_float(cv[0][0]) + __uint_as_float(cv[1][0])) + (__uint_as_float(cv[2][0]) + __uint_as_float(cv[3][0]))) + __uint_as_float(cv[4][0]);
        }
        if (t + 1 < A_NT) lds_bar();
        if (t == A_NT - 4) gather();
        if (t == 3) addresses();
        step(F1, F0, t + 1, 0, t + 1 < A_NT, t == 0);
        if (t == 0 && rb < A_NT && h == 0) put_granule(p, WS_SC + (t0 * 3 * DM + col0 + cb0 + r) * 8, tag, csum[0]);
    }
    if (ISV) {
#pragma unroll
        for (int a = 0; a < 2; ++a) {
            const int tb = a ? tb1 : tb0;
            f32x4 av[4], bv[4];
#pragma unroll
            for (int gg = 0; gg < 4; ++gg) { av[gg] = *(const LAS f32x4*)(sa + tb + 8 * gg + 4 * h); bv[gg] = *(const LAS f32x4*)(sb + tb + 8 * gg + 4 * h); }
#pragma unroll
            for (int j = 0; j < 2; ++j) {
                const int cl = (j ? cb1 : cb0) + r;
                const float cs = cst[cl] + cst[128 + cl];
                const unsigned dst = eo[j] + (tb >> 5) * 3072;
#pragma unroll
                for (int s = 0; s < 2; ++s) {
                    u32x4 v;
#pragma unroll
                    for (int i = 0; i < 4; ++i) {
                        const int e = 8 * s + 2 * i;
                        v[i] = cvtpk(av[e >> 2][e & 3] * acc[a][j][e] + bv[e >> 2][e & 3] * cs, av[e >> 2][(e & 3) + 1] * acc[a][j][e + 1] + bv[e >> 2][(e & 3) + 1] * cs);
                    }
                    st128(rw, dst + s * 768, v);
                }
            }
        }
    } else {
        const unsigned ts = ct < 5 ? 1280 : 3072;
        const float ra0 = sa[tb0 + r], rb0 = sb[tb0 + r], ra1 = sa[tb1 + r], rb1 = sb[tb1 + r];
#pragma unroll
        for (int j = 0; j < 2; ++j)
#pragma unroll
            for (int gg = 0; gg < 4; ++gg) {
                const int cl = (j ? cb1 : cb0) + 8 * gg + 4 * h;
                const f32x4 cs = *(const LAS f32x4*)(cst + cl) + *(const LAS f32x4*)(cst + 128 + cl);
                const unsigned off = eo[4 * j + gg];
#pragma unroll
                for (int a = 0; a < 2; ++a) {
                    const float ra = a ? ra1 : ra0, rbb = a ? rb1 : rb0;
                    u32x2 v;
                    v[0] = cvtpk(ra * acc[a][j][4 * gg] + rbb * cs[0], ra * acc[a][j][4 * gg + 1] + rbb * cs[1]);
                    v[1] = cvtpk(ra * acc[a][j][4 * gg + 2] + rbb * cs[2], ra * acc[a][j][4 * gg + 3] + rbb * cs[3]);
                    st64(rw, off + ((a ? tb1 : tb0) >> 5) * ts, v);
                }
            }
    }
    wait_bar<0>();
}
DI void phaseA(const Params& p, LAS char* sm, int b, unsigned ep, unsigned& tag) {
    const int g = b & 7, i = b >> 3, rb = 4 * (g & 3) + (i & 3), ct = (g >> 2) * 8 + (i >> 2);
    if (ct >= 15) {
        const int l = threadIdx.x & 63, w = uni(threadIdx.x >> 6);
        StatsA sta;
        if (w < 4) sta.request(p, rb, ct, w, l);
        tag = tag_of(ep);
        if (w < 4) sta.tokens(p, rb, ct, w, l, tag);
        return;
    }
    if (ct >= 10) phaseA_body<true>(p, sm, rb, ct, ep, tag); else phaseA_body<false>(p, sm, rb, ct, ep, tag);
}

constexpr int B_STAGE = 8 * 3072, B_NS = 3, B_NT = 8, B_FLAG = B_STAGE * B_NS;
constexpr int B_SCR = B_FLAG + 64;
constexpr int C_WO = 75776, C_WO4 = 49152;

DI u32x2 neg_split3(float m) {
    const unsigned hi = cvtpk(m, 0.f) & 0xffffu; const float r1 = m - bf_lo(hi);
    const unsigned mid = cvtpk(r1, 0.f) & 0xffffu; const float r2 = r1 - bf_lo(mid);
    const unsigned lo = cvtpk(r2, 0.f) & 0xffffu;
    return u32x2{(hi | (mid << 16)) ^ 0x80008000u, lo ^ 0x8000u};
}
DI float max16(const f32x16& s) {
    float a = __builtin_fmaxf(__builtin_fmaxf(s[0], s[1]), s[2]), b = __builtin_fmaxf(__builtin_fmaxf(s[3], s[4]), s[5]);
    a = __builtin_fmaxf(__builtin_fmaxf(a, s[6]), s[7]); b = __builtin_fmaxf(__builtin_fmaxf(b, s[8]), s[9]);
    a = __builtin_fmaxf(__builtin_fmaxf(a, s[10]), s[11]); b = __builtin_fmaxf(__builtin_fmaxf(b, s[12]), s[13]);
    a = __builtin_fmaxf(__builtin_fmaxf(a, s[14]), s[15]);
    return __builtin_fmaxf(a, b);
}
DI bf16x4 ld64(const LAS char* p) { return *(const LAS bf16x4*)p; }

DI void phaseB(const Params& p, LAS char* sm, int b, unsigned tag) {
    const int tid = threadIdx.x, l = tid & 63, w = uni(tid >> 6), r = l & 31, h = l >> 5;
    const int g = b & 7, i = b >> 3, head = 4 * g + (i >> 3);
    const int kh = w >> 2, TA = (i & 7) * 8 + (w & 3) * 2;
    const unsigned sm0 = (unsigned)(uintptr_t)sm;
    const rsrc_t rs = mk_rsrc(p.ws + WS_KV + (size_t)head * 64 * 3072, 64 * 3072);
    const unsigned voff = (kh * 32 + (w & 3)) * 3072 + l * 16;
    auto issue = [&](int t, int slot) {
#pragma unroll
        for (int j = 0; j < 3; ++j) dma16<true>(sm0 + slot * B_STAGE + w * 3072 + j * 1024, voff, rs, t * 4 * 3072 + j * 1024);
    };
    const unsigned w2o = l < 24 ? b * 24576 + (w * 24 + l) * 128 : 0xffffff00u;
    const unsigned w1i = (i * 8 + w) * 48 + l, w1o = l < 48 ? (w1i / 6) * (FF * 2) + (g >> 1) * 768 + (w1i % 6) * 128 : 0xffffff00u;
    const unsigned w3o = l < 32 ? ((l >> 1) & 1) * (SEQ * DM * 2) + ((g * 4 + (i >> 3)) * 64 + w * 8 + (l >> 2)) * (DM * 2) + (((i & 7) * 160) & ~127) + (l & 1) * 128 : 0xffffff00u;
    const rsrc_t rwo = mk_rsrc(p.wo, DM * DM * 2);
    auto wo_piece = [&](unsigned lds, int u, int row0) {
        const int ch = u * 64 + l, kr = ch / 10, c = ch - kr * 10;
        dma16<false>(lds, (row0 + kr) * (DM * 2) + (i & 7) * 160 + c * 16, rwo, 0);
    };
    unsigned wov[10];
#pragma unroll
    for (int k = 0; k < 10; ++k) { const int ch = (w * 10 + k) * 64 + l, kr = ch / 10; wov[k] = kr * (DM * 2) + (i & 7) * 160 + (ch - kr * 10) * 16; }
    asm volatile("" : "+v"(wov[0]), "+v"(wov[1]), "+v"(wov[2]), "+v"(wov[3]), "+v"(wov[4]), "+v"(wov[5]), "+v"(wov[6]), "+v"(wov[7]), "+v"(wov[8]), "+v"(wov[9]));
    if (w == 0) {
        const int c0 = head * 20, rep = (i & (NREP - 1)) * 1024;
        const int ctl = (l < 32 ? DM : 2 * DM) + c0 + ((l >> 4) & 1) * 19;
        const unsigned off[2] = {WS_FA + rep + ((ctl >> 7) * 16 + (l & 15)) * 4,
                                 WS_FA + rep + (((c0 + (l & 1) * 19) >> 7) * 16 + (i & 7) * 2 + ((l >> 1) & 1)) * 4};
        poll<2>(p, off, tag);
    }
    lds_bar();
    u32x2 qp[6];
    {
        const rsrc_t rq = mk_rsrc(p.ws + WS_Q, NH * 64 * 1280);
        const unsigned q0o = (head * 64 + TA) * 1280 + h * 512 + r * 8, q1o = (head * 64 + TA) * 1280 + 1024 + r * 8;
        asm volatile("buffer_load_dwordx2 %0, %6, %8, 0 offen sc1\n\tbuffer_load_dwordx2 %1, %6, %8, 0 offen offset:256 sc1\n\t"
                     "buffer_load_dwordx2 %2, %6, %8, 0 offen offset:1280 sc1\n\tbuffer_load_dwordx2 %3, %6, %8, 0 offen offset:1536 sc1\n\t"
                     "buffer_load_dwordx2 %4, %7, %8, 0 offen sc1\n\tbuffer_load_dwordx2 %5, %7, %8, 0 offen offset:1280 sc1"
                     : "=&v"(qp[0]), "=&v"(qp[1]), "=&v"(qp[2]), "=&v"(qp[3]), "=&v"(qp[4]), "=&v"(qp[5]) : "v"(q0o), "v"(q1o), "s"(rq) : "memory");
    }
    issue(0, 0);
    asm volatile("s_waitcnt vmcnt(0)" : "+v"(qp[0]), "+v"(qp[1]), "+v"(qp[2]), "+v"(qp[3]), "+v"(qp[4]), "+v"(qp[5])::"memory");
    issue(1, 1);
    const bf16x8 qA0 = __builtin_bit_cast(bf16x8, u32x4{qp[0][0], qp[0][1], qp[1][0], qp[1][1]}), qB0 = __builtin_bit_cast(bf16x8, u32x4{qp[2][0], qp[2][1], qp[3][0], qp[3][1]});
    u32x2 qA1u = qp[4], qB1u = qp[5];
    if (h) { qA1u = u32x2{0u, 0u}; qB1u = u32x2{0u, 0u}; }
    const unsigned k0o = h * 512 + r * 8, k1o = 1024 + l * 8, vo = 1536 + h * 384 + (r < 22 ? r : 22) * 16;
    auto ldk0 = [&](const LAS char* tp) { const bf16x4 a = ld64(tp + k0o), c = ld64(tp + k0o + 256); return __builtin_shufflevector(a, c, 0, 1, 2, 3, 4, 5, 6, 7); };
    const LAS char* sw = sm + kh * 4 * 3072;
    f32x16 oA, oB;
    float lsum, mA, mB;
    auto scores = [&](const LAS char* tp, bf16x8 q0, u32x2 q1) {
        return mfma32k8(ld64(tp + k1o), __builtin_bit_cast(bf16x4, q1), mfma32(ldk0(tp), q0, f32x16{}));
    };
    for (int pass = 0;; ++pass) {
        if (pass == 1) {
            if (h) { qA1u = u32x2{0u, 0u}; qB1u = u32x2{0u, 0u}; }
            mA = -INFINITY; mB = -INFINITY;
            issue(0, 0); issue(1, 1);
            for (int t = 0; t < B_NT; ++t) {
                if (t + 1 < B_NT) wait_bar<3>(); else wait_bar<0>();
                if (t + 2 < B_NT) issue(t + 2, (t + 2) % B_NS);
                const LAS char* st = sm + (t % B_NS) * B_STAGE;
#pragma unroll 1
                for (int kt = 0; kt < 8; ++kt) {
                    mA = __builtin_fmaxf(mA, max16(scores(st + kt * 3072, qA0, qA1u)));
                    mB = __builtin_fmaxf(mB, max16(scores(st + kt * 3072, qB0, qB1u)));
                }
            }
            mA = xhalf_max(mA); mB = xhalf_max(mB);
            lds_bar();
            if (h) { qA1u = neg_split3(mA); qB1u = neg_split3(mB); }
            issue(0, 0); issue(1, 1);
        }
        wait_bar<3>();
        if (pass == 0) {
            const f32x16 s0 = scores(sw, qA0, qA1u), s1 = scores(sw, qB0, qB1u);
            const unsigned mm2 = cvtpk(xhalf_max(max16(s0)), xhalf_max(max16(s1)));
            mA = bf_lo(mm2); mB = bf_hi(mm2);
            if (h) { qA1u = u32x2{(mm2 & 0xffffu) ^ 0x8000u, 0u}; qB1u = u32x2{(mm2 >> 16) ^ 0x8000u, 0u}; }
        }
        if (w >= 4) __builtin_amdgcn_s_setprio(1);
        const bf16x4 qA1 = __builtin_bit_cast(bf16x4, qA1u), qB1 = __builtin_bit_cast(bf16x4, qB1u);
        oA = f32x16{}; oB = f32x16{};
        bf16x8 kf0 = ldk0(sw), vfa = ld128(sw + vo), vfb, vf1 = bf16x8{};
        bf16x4 kf1 = ld64(sw + k1o);
        f32x16 sA = mfma32k8(kf1, qA1, mfma32(kf0, qA0, f32x16{})), sB;
        u32x4 p0, p1 = u32x4{0u, 0u, 0u, 0u};
#define EX(v) __builtin_amdgcn_exp2f(v)
#define BF(P) __builtin_bit_cast(bf16x8, P)
#define GRP(S, a, P, c) do { \
            const float e0_ = EX(S[a]), e1_ = EX(S[a + 1]), e2_ = EX(S[a + 2]), e3_ = EX(S[a + 3]); \
            P[c] = cvtpk(e0_, e1_); P[c + 1] = cvtpk(e2_, e3_); PIN(P); SBAR(); } while (0)
#define MF(stmt) do { stmt; SBAR(); } while (0)
#define TILE(TC, TN, V0, V0N) do { \
            MF(sB = mfma32(kf0, qB0, f32x16{}));          GRP(sA, 0, p0, 0); \
            MF(oB = mfma32(vf1, BF(p1), oB)); \
            MF((kf0 = ldk0(TN), vf1 = ld128((TC) + vo + 768), V0N = ld128((TN) + vo))); \
                                                          GRP(sA, 4, p0, 2); \
            MF(sB = mfma32k8(kf1, qB1, sB)); \
            MF(kf1 = ld64((TN) + k1o));                   GRP(sA, 8, p1, 0); \
            MF(oA = mfma32(V0, BF(p0), oA));              GRP(sA, 12, p1, 2); \
            asm volatile("" : "+v"(kf0), "+v"(kf1), "+v"(vf1), "+v"(V0N)); SBAR(); \
            MF(sA = mfma32(kf0, qA0, f32x16{}));          GRP(sB, 0, p0, 0); \
            MF(oA = mfma32(vf1, BF(p1), oA));             GRP(sB, 4, p0, 2); \
            MF(sA = mfma32k8(kf1, qA1, sA));              GRP(sB, 8, p1, 0); \
            MF(oB = mfma32(V0, BF(p0), oB));              GRP(sB, 12, p1, 2); } while (0)
        auto extras = [&](int t) {
            if (t < 5) { dma16<false>(sm0 + C_WO + (w * 10 + 2 * t) * 1024, wov[2 * t], rwo, 0); dma16<false>(sm0 + C_WO + (w * 10 + 2 * t + 1) * 1024, wov[2 * t + 1], rwo, 0); }
            if (t == W2T) warm_lds(sm0 + B_SCR, w2o, mk_rsrc(p.w2, SEQ * FF * 2));
            if (t == W1T) warm_lds(sm0 + B_SCR, w1o, mk_rsrc(p.w1, SEQ * FF * 2));
            if (t == W3T) warm_lds(sm0 + B_SCR, w3o, mk_rsrc(p.ws + WS_H, WS_U - WS_H));
        };
#pragma unroll
        for (int t = 0; t < B_NT; ++t) {
            const LAS char* st = sw + (t % B_NS) * B_STAGE;
            const LAS char* sn = sw + ((t + 1) % B_NS) * B_STAGE;
            TILE(st, st + 1 * 3072, vfa, vfb);
            TILE(st + 1 * 3072, st + 2 * 3072, vfb, vfa);
            TILE(st + 2 * 3072, st + 3 * 3072, vfa, vfb);
            if (t + 1 < B_NT) {
                { const int pv = t - 1, n = t == 0 ? 0 : (pv < 5 ? 2 : 0) + (pv == W2T) + (pv == W1T) + (pv == W3T);
                  if (n == 0) wait_bar<0>(); else if (n == 1) wait_bar<1>(); else if (n == 2) wait_bar<2>(); else if (n == 3) wait_bar<3>(); else wait_bar<4>(); }
                if (t + 2 < B_NT) issue(t + 2, (t + 2) % B_NS);
                extras(t);
            } else wait_bar<0>();
            TILE(st + 3 * 3072, sn, vfb, vfa);
        }
        oB = mfma32(vf1, BF(p1), oB);
        __builtin_amdgcn_s_setprio(0);
#undef TILE
#undef MF
#undef GRP
#undef BF
#undef EX
        LAS f32x4* red = (LAS f32x4*)sm + (w & 3) * 6 * 64 + l;
        LAS int* flag = (LAS int*)(sm + B_FLAG);
        bool ok = true;
        if (pass == 0) {
#pragma unroll
            for (int e = 0; e < 12; ++e) ok = ok && (e > 8 && h ? true : __builtin_fabsf(oA[e]) < 0x1p126f && __builtin_fabsf(oB[e]) < 0x1p126f);
            ok = ok && (!h || (oA[8] > 0x1p-30f && oA[8] < 0x1p63f && oB[8] > 0x1p-30f && oB[8] < 0x1p63f));
        }
        {
            f32x16 give = kh ? oA : oB;
            if (h) give[9] = kh ? mA : mB;
#pragma unroll
            for (int e = 0; e < 3; ++e) red[(kh * 3 + e) * 64] = f32x4{give[4 * e], give[4 * e + 1], give[4 * e + 2], give[4 * e + 3]};
            const bool wave_ok = __all(ok);
            if (l == 0) flag[w] = wave_ok ? 0 : 1;
        }
        lds_bar();
        f32x16 o = kh ? oB : oA;
        {
            f32x4 v[3];
#pragma unroll
            for (int e = 0; e < 3; ++e) v[e] = red[((kh ^ 1) * 3 + e) * 64];
            auto rm = __builtin_amdgcn_permlane32_swap(__float_as_uint(v[2][1]), __float_as_uint(v[2][1]), false, false);
            const float mo = __uint_as_float(rm[1]), mm = kh ? mB : mA, mx = __builtin_fmaxf(mo, mm);
            const float fo = __builtin_amdgcn_exp2f(mo - mx), fm = __builtin_amdgcn_exp2f(mm - mx);
#pragma unroll
            for (int e = 0; e < 3; ++e)
#pragma unroll
                for (int k = 0; k < 4; ++k) o[4 * e + k] = o[4 * e + k] * fm + v[e][k] * fo;
        }
        oA = o;
        {
            auto rr = __builtin_amdgcn_permlane32_swap(__float_as_uint(o[8]), __float_as_uint(o[8]), false, false);
            lsum = __uint_as_float(rr[1]);
        }
        const u32x4 f0 = *(LAS u32x4*)flag, f1 = *(LAS u32x4*)(flag + 4);
        const bool redo = uni(f0[0] | f0[1] | f0[2] | f0[3] | f1[0] | f1[1] | f1[2] | f1[3]);
        if (!redo) break;
        lds_bar();
    }
    const float inv = __builtin_amdgcn_rcpf(lsum);
    const rsrc_t rw = mk_rsrc(p.ws, WS_END);
    const unsigned oo = WS_O + (head * 5 + h) * (SEQ * 8) + ((TA + kh) * 32 + r) * 8;
#pragma unroll
    for (int e = 0; e < 3; ++e) {
        if (e < 2 || h == 0) {
            u32x2 v;
            v[0] = cvtpk(oA[4 * e] * inv, oA[4 * e + 1] * inv); v[1] = cvtpk(oA[4 * e + 2] * inv, oA[4 * e + 3] * inv);
            st64(rw, oo + 2 * e * (SEQ * 8), v);
        }
    }
    wait_bar<0>();
    if (w == 4) publish(p, WS_FB + ((i & 7) * 32 + head) * 4, 1024, tag, l);
    if (w >= 6) {
#pragma unroll
        for (int j = 0; j < 10; ++j) wo_piece(sm0 + C_WO4 + ((w - 6) * 10 + j) * 1024, (w - 6) * 10 + j, 512);
    }
}

constexpr int C_NT = 5;
constexpr int D_NT = 16, D_W = 65536;
constexpr int E_NT = 12, E_W = 81920;

DI void tileD(int b, int& db, int& fb) { const int gx = b & 7, ix = b >> 3; db = 5 * (gx & 1) + ix % 5; fb = 6 * (gx >> 1) + ix / 5; }
struct W1Stream {
    rsrc_t rs; unsigned voff, ldst;
    DI W1Stream(const Params& p, unsigned sm0, int fb, int w, int l) {
        rs = mk_rsrc(p.w1, SEQ * FF * 2);
        const int kr = 64 * (w & 1) + (l >> 3);
        voff = kr * (FF * 2) + fb * 128 + (((l & 7) ^ (((kr >> 1) & 1) << 2)) << 4);
        ldst = sm0 + D_W + (w & 1) * 8192;
    }
    DI void operator()(int t) const {
#pragma unroll
        for (int j = 0; j < 8; ++j) dma16<false>(ldst + (t % 6) * 16384 + j * 1024, voff, rs, (unsigned)((t * 128 + j * 8) * (FF * 2)));
    }
};
struct W2Stream {
    rsrc_t rs; unsigned voff[2], ldst;
    DI W2Stream(const Params& p, unsigned sm0, int s0, int w, int l) {
        rs = mk_rsrc(p.w2, FF * SEQ * 2);
#pragma unroll
        for (int j = 0; j < 2; ++j) {
            const int kr = 8 * ((w & 1) * 8 + j) + (l >> 3), fr = ((kr >> 1) & 1) | (((kr >> 3) & 1) << 1);
            voff[j] = kr * (SEQ * 2) + s0 * 2 + (((l & 7) ^ (fr << 1)) << 4);
        }
        ldst = sm0 + E_W + (w & 1) * 8192;
    }
    DI void operator()(int t) const {
#pragma unroll
        for (int j = 0; j < 8; ++j) dma16<false>(ldst + (t % 5) * 16384 + j * 1024, voff[j & 1], rs, (unsigned)((t * 128 + (j >> 1) * 16) * (SEQ * 2)));
    }
};

DI void phaseC(const Params& p, LAS char* sm, int b, unsigned tag) {
    const int tid = threadIdx.x, l = tid & 63, w = uni(tid >> 6);
    const int gx = b & 7, ix = b >> 3, rb = 4 * gx + (ix >> 3), cb = ix & 7;
    const int row0 = rb * 64, col0 = cb * 80;
    const unsigned sm0 = (unsigned)(uintptr_t)sm;
    if (w >= 6) {
        wait_bar<0>();
#pragma unroll
        for (int t = 1; t < C_NT; ++t) lds_bar();
        lds_bar();
        lds_bar();
        if (ix < 30) {
            int db, fb; tileD(b, db, fb);
            const W1Stream w1(p, sm0, fb, w, l);
#pragma unroll
            for (int t = 0; t < PW1; ++t) w1(t);
        }
        return;
    }
    if (w >= 4) {
        const rsrc_t rsO = mk_rsrc(p.ws + WS_O, SEQ * DM * 2);
        const unsigned vo = ((w & 1) * 16 + (l >> 5)) * (SEQ * 8) + (row0 + 2 * ((l & 31) ^ ((l >> 5) << 3))) * 8;
        auto issue = [&](int t) {
#pragma unroll
            for (int j = 0; j < 8; ++j) dma16<true>(sm0 + (t % 3) * 16384 + ((w & 1) * 8 + j) * 1024, vo, rsO, (t * 32 + 2 * j) * (SEQ * 8));
        };
        { const unsigned off[1] = {WS_FB + (ix & (NREP - 1)) * 1024 + (gx * 32 + (l & 31)) * 4}; poll<1>(p, off, tag); }
        issue(0); issue(1); issue(2);
        ring_loader<8, C_NT, 3, 3, 3>(issue);
        lds_bar();
        lds_bar();
        if (w == 4) publish(p, WS_FC + (cb * 32 + rb) * 4, 1024, tag, l);
        return;
    }
    const int tk = l & 15, g = l >> 4, q = (l & 15) >> 2, p4 = l & 3;
    const int row = row0 + 16 * w + tk;
    const rsrc_t rw = mk_rsrc(p.ws, WS_END);
    u32x2 xv[5];
    {
        const rsrc_t rx = mk_rsrc(p.x, SEQ * DM * 2);
#pragma unroll
        for (int j = 0; j < 5; ++j) xv[j] = ld64g<0>(rx, (row * DM + col0 + 16 * j + 4 * g) * 2);
    }
    const unsigned oa = g * 512 + (((16 * w + tk) * 8) ^ ((g & 1) << 7));
    const unsigned wa = (4 * g + q) * 160 + p4 * 8;
    f32x4 acc[5] = {};
    struct Frag { bf16x8 o, w[5]; } F0, F1;
    const Anch ao = mk_anch(sm0 + oa), aw = mk_anch(sm0 + wa);
    auto step = [&](const Frag& C, Frag& N, int t, int ks, bool more) {
        const int so = (t % 3) * 16384 + ks * 4096, sw = (t < 4 ? C_WO + t * 20480 : C_WO4) + ks * 32 * 160;
        if (more) N.o = __builtin_shufflevector(ld64(at(ao, so)), ld64(at(ao, so + 2048)), 0, 1, 2, 3, 4, 5, 6, 7);
        SBAR();
#pragma unroll
        for (int j = 0; j < 5; ++j) {
            acc[j] = mfma16(C.w[j], C.o, acc[j]); SBAR();
            if (more) N.w[j] = ldtr2(at(aw, sw + j * 32), at(aw, sw + j * 32 + 16 * 160));
            SBAR();
        }
    };
    lds_bar();
    F0.o = __builtin_shufflevector(ld64(at(ao, 0)), ld64(at(ao, 2048)), 0, 1, 2, 3, 4, 5, 6, 7);
#pragma unroll
    for (int j = 0; j < 5; ++j) F0.w[j] = ldtr2(at(aw, C_WO + j * 32), at(aw, C_WO + j * 32 + 16 * 160));
#pragma unroll
    for (int t = 0; t < C_NT; ++t) {
        step(F0, F1, t, 1, true);
        step(F1, F0, t, 2, true);
        step(F0, F1, t, 3, true);
        lds_bar();
        step(F1, F0, t + 1, 0, t + 1 < C_NT);
    }
    float s = 0.f, ss = 0.f, hv[20];
#pragma unroll
    for (int j = 0; j < 5; ++j) {
        hv[4 * j] = acc[j][0] + bf_lo(xv[j][0]); hv[4 * j + 1] = acc[j][1] + bf_hi(xv[j][0]); hv[4 * j + 2] = acc[j][2] + bf_lo(xv[j][1]); hv[4 * j + 3] = acc[j][3] + bf_hi(xv[j][1]);
    }
    u32x2 hb[5];
#pragma unroll
    for (int j = 0; j < 5; ++j) {
        hb[j][0] = cvtpk(hv[4 * j], hv[4 * j + 1]); hb[j][1] = cvtpk(hv[4 * j + 2], hv[4 * j + 3]);
        hv[4 * j] = bf_lo(hb[j][0]); hv[4 * j + 1] = bf_hi(hb[j][0]); hv[4 * j + 2] = bf_lo(hb[j][1]); hv[4 * j + 3] = bf_hi(hb[j][1]);
    }
    {
        float s2[4] = {0.f, 0.f, 0.f, 0.f}, q2[4] = {0.f, 0.f, 0.f, 0.f};
#pragma unroll
        for (int j = 0; j < 5; ++j)
#pragma unroll
            for (int e = 0; e < 4; ++e) { s2[e] += hv[4 * j + e]; q2[e] += hv[4 * j + e] * hv[4 * j + e]; }
        s = (s2[0] + s2[1]) + (s2[2] + s2[3]); ss = (q2[0] + q2[1]) + (q2[2] + q2[3]);
    }
    s = xhalf_sum(xrow_sum(s)); ss = xhalf_sum(xrow_sum(ss));
    GAS unsigned long long* gran = (GAS unsigned long long*)(p.ws + WS_ST) + (size_t)rb * 8 * 64 * 2 + (16 * w + tk) * 2;
    if (g < 2) __hip_atomic_store(gran + cb * 128 + g, ((unsigned long long)tag << 32) | __float_as_uint(g == 0 ? s : ss), __ATOMIC_RELAXED, __HIP_MEMORY_SCOPE_AGENT);
    LAS char* stg = sm + w * 2560;
    auto put_rows = [&](const u32x2 (&v)[5], unsigned base) {
#pragma unroll
        for (int j = 0; j < 5; ++j) *(LAS u32x2*)(stg + tk * 160 + j * 32 + g * 8) = v[j];
        asm volatile("s_waitcnt lgkmcnt(0)" ::: "memory");
#pragma unroll
        for (int j = 0; j < 3; ++j) {
            const int ch = j * 64 + l, rr = ch / 10, c = ch - rr * 10;
            if (j < 2 || l < 32) st128(rw, base + ((row0 + 16 * w + rr) * DM + col0) * 2 + c * 16, *(const LAS u32x4*)(stg + ch * 16));
        }
        asm volatile("s_waitcnt lgkmcnt(0)" ::: "memory");
    };
    put_rows(hb, WS_H);
    {
        unsigned long long a0, a1, c0, c1;
        for (unsigned spins = 0; spins < (1u << 16); ++spins) {
            a0 = __hip_atomic_load(gran + (2 * g) * 128, __ATOMIC_RELAXED, __HIP_MEMORY_SCOPE_AGENT);
            c0 = __hip_atomic_load(gran + (2 * g) * 128 + 1, __ATOMIC_RELAXED, __HIP_MEMORY_SCOPE_AGENT);
            a1 = __hip_atomic_load(gran + (2 * g + 1) * 128, __ATOMIC_RELAXED, __HIP_MEMORY_SCOPE_AGENT);
            c1 = __hip_atomic_load(gran + (2 * g + 1) * 128 + 1, __ATOMIC_RELAXED, __HIP_MEMORY_SCOPE_AGENT);
            if (__all((unsigned)(a0 >> 32) == tag && (unsigned)(c0 >> 32) == tag && (unsigned)(a1 >> 32) == tag && (unsigned)(c1 >> 32) == tag)) break;
            __builtin_amdgcn_s_sleep(1);
        }
        s = __uint_as_float((unsigned)a0) + __uint_as_float((unsigned)a1); ss = __uint_as_float((unsigned)c0) + __uint_as_float((unsigned)c1);
        s = xhalf_sum(xrow_sum(s)); ss = xhalf_sum(xrow_sum(ss));
    }
    {
        const float m = s * (1.f / DM), var = fmaxf(ss * (1.f / DM) - m * m, 0.f);
        const float r1 = __builtin_amdgcn_rsqf(var + EPS);
        const float r2 = __builtin_amdgcn_rsqf(var * r1 * r1 + EPS), rr = r1 * r2;
        u32x2 v[5];
#pragma unroll
        for (int j = 0; j < 5; ++j) {
            v[j][0] = cvtpk((hv[4 * j] - m) * rr, (hv[4 * j + 1] - m) * rr); v[j][1] = cvtpk((hv[4 * j + 2] - m) * rr, (hv[4 * j + 3] - m) * rr);
        }
        put_rows(v, WS_HN);
    }
    wait_bar<0>();
}

DI float gelu_tanh(float v) {
    const float y = v * (0.7978845608028654f + 0.7978845608028654f * 0.044715f * v * v);
    return v * __builtin_amdgcn_rcpf(1.f + __builtin_amdgcn_exp2f(-2.f * 1.4426950408889634f * y));
}
DI void phaseD(const Params& p, LAS char* sm, int b, unsigned tag) {
    const int gx = b & 7, ix = b >> 3;
    const int tid = threadIdx.x, l = tid & 63, w = uni(tid >> 6);
    const unsigned sm0 = (unsigned)(uintptr_t)sm;
    const int e_s0 = (8 * (gx >> 1) + (ix >> 2)) * 64;
    if (ix >= 30) {
        if (w >= 6) { const W2Stream w2(p, sm0, e_s0, w, l); for (int t = 0; t < PW2; ++t) w2(t); }
        return;
    }
    int db, fb; tileD(b, db, fb);
    if (w >= 6) {
        const W1Stream w1(p, sm0, fb, w, l);
        ring_loader<8, D_NT, 6, PW1, 4>(w1);
        lds_bar();
        lds_bar();
        const W2Stream w2(p, sm0, e_s0, w, l);
#pragma unroll
        for (int t = 0; t < PW2; ++t) w2(t);
        return;
    }
    if (w >= 4) {
        const rsrc_t rs = mk_rsrc(p.ws + WS_HN, SEQ * DM * 2);
        const int kr = 64 * (w & 1) + (l >> 3);
        const unsigned voff = kr * (DM * 2) + db * 128 + (((l & 7) ^ (((kr >> 1) & 1) << 2)) << 4);
        auto issue = [&](int t) {
#pragma unroll
            for (int j = 0; j < 8; ++j) dma16<true>(sm0 + (t & 3) * 16384 + (w & 1) * 8192 + j * 1024, voff, rs, (unsigned)((t * 128 + j * 8) * (DM * 2)));
        };
        { const unsigned off[1] = {WS_FC + (ix & (NREP - 1)) * 1024 + (((db * 64 + (l >> 5) * 63) / 80) * 32 + (l & 31)) * 4}; poll<1>(p, off, tag); }
        issue(0); issue(1);
        ring_loader<8, D_NT, 4, 2, 4>(issue);
        lds_bar();
        lds_bar();
        if (w == 4) publish(p, WS_FD + (db * 32 + fb) * 4, 1280, tag, l);
        return;
    }
    const int wf = w >> 1, wd = w & 1, r = l & 31, h = l >> 5;
    const int g = l >> 4, q = (l & 15) >> 2, p4 = l & 3;
    u32x2 bv[4];
    {
        const rsrc_t rb1 = mk_rsrc(p.b1, FF * 2);
#pragma unroll
        for (int e = 0; e < 4; ++e) bv[e] = ld64g<0>(rb1, (fb * 64 + 32 * wf + 8 * e + 4 * h) * 2);
    }
    const unsigned rowo = (8 * (g >> 1) + q) * 128 + (p4 & 1) * 8, sw = ((q >> 1) & 1) << 2;
    const unsigned ha = rowo + (((4 * wd + 2 * (g & 1) + (p4 >> 1)) ^ sw) << 4);
    const unsigned wa = rowo + (((4 * wf + 2 * (g & 1) + (p4 >> 1)) ^ sw) << 4);
    f32x16 acc = {};
    struct Frag { bf16x8 h, w; } F0, F1;
    const Anch ah = mk_anch(sm0 + ha), aw = mk_anch(sm0 + wa);
    auto rd = [&](Frag& F, int t, int ks) {
        const int sh = (t & 3) * 16384 + ks * 2048, sv = D_W + (t % 6) * 16384 + ks * 2048;
        F.h = ldtr2(at(ah, sh), at(ah, sh + 512));
        F.w = ldtr2(at(aw, sv), at(aw, sv + 512));
        SBAR();
    };
    auto mm = [&](const Frag& F) {
        acc = mfma32(F.w, F.h, acc); SBAR(); };
    lds_bar();
    rd(F0, 0, 0);
#pragma unroll
    for (int t = 0; t < D_NT; ++t) {
        rd(F1, t, 1); mm(F0);
        rd(F0, t, 2); mm(F1);
        rd(F1, t, 3); mm(F0);
        rd(F0, t, 4); mm(F1);
        rd(F1, t, 5); mm(F0);
        rd(F0, t, 6); mm(F1);
        rd(F1, t, 7); mm(F0);
        lds_bar();
        if (t + 1 < D_NT) rd(F0, t + 1, 0);
        mm(F1);
    }
    {
        LAS char* stg = sm + w * 2048;
#pragma unroll
        for (int e = 0; e < 4; ++e) {
            u32x2 v;
            v[0] = cvtpk(gelu_tanh(acc[4 * e] + bf_lo(bv[e][0])), gelu_tanh(acc[4 * e + 1] + bf_hi(bv[e][0])));
            v[1] = cvtpk(gelu_tanh(acc[4 * e + 2] + bf_lo(bv[e][1])), gelu_tanh(acc[4 * e + 3] + bf_hi(bv[e][1])));
            *(LAS u32x2*)(stg + r * 64 + ((e ^ ((r >> 1) & 3)) << 4) + h * 8) = v;
        }
        asm volatile("s_waitcnt lgkmcnt(0)" ::: "memory");
        const rsrc_t rw = mk_rsrc(p.ws, WS_END);
#pragma unroll
        for (int j = 0; j < 2; ++j) {
            const int rr = 16 * j + (l >> 2), c = l & 3;
            st128(rw, WS_U + ((db * 64 + 32 * wd + rr) * FF + fb * 64 + 32 * wf) * 2 + c * 16, *(const LAS u32x4*)(stg + rr * 64 + ((c ^ ((rr >> 1) & 3)) << 4)));
        }
    }
    wait_bar<0>();
}

DI void phaseE(const Params& p, LAS char* sm, int b, unsigned tag) {
    const int gx = b & 7, ix = b >> 3, db = 4 * (gx & 1) + (ix & 3), sb = 8 * (gx >> 1) + (ix >> 2);
    const int d0 = db * 80, s0 = sb * 64;
    const int tid = threadIdx.x, l = tid & 63, w = uni(tid >> 6);
    const unsigned sm0 = (unsigned)(uintptr_t)sm;
    if (w >= 6) {
        const W2Stream w2(p, sm0, s0, w, l);
        ring_loader<8, E_NT, 5, PW2, 4>(w2);
        return;
    }
    const unsigned fdo[1] = {WS_FD + (ix & (NREP - 1)) * 1280 + (((d0 + (l >> 5) * 79) >> 6) * 32 + ((l & 31) < 24 ? (l & 31) : 0)) * 4};
    if (w >= 4) {
        const rsrc_t rsU = mk_rsrc(p.ws + WS_U, DM * FF * 2);
        unsigned vu[4];
#pragma unroll
        for (int j = 0; j < 4; ++j) { const int rr = 4 * ((w & 1) * 10 + j) + (l >> 4), c = (l & 15) ^ (rr & 15); vu[j] = (d0 + rr) * (FF * 2) + c * 16; }
        auto issue = [&](int t) {
#pragma unroll
            for (int j = 0; j < 10; ++j) dma16<true>(sm0 + (t & 3) * 20480 + ((w & 1) * 10 + j) * 1024, vu[j & 3], rsU, t * 256 + (j >> 2) * 16 * (FF * 2));
        };
        poll<1>(p, fdo, tag);
        issue(0); issue(1);
        ring_loader<10, E_NT, 4, 2, 4>(issue);
        return;
    }
    const int tk = l & 15, g = l >> 4, q = (l & 15) >> 2, p4 = l & 3;
    const int srow = s0 + 16 * w + tk;
    const int fr = ((q >> 1) & 1) | ((g & 1) << 1);
    const unsigned wa = (8 * g + q) * 128 + (((2 * w + (p4 >> 1)) ^ (fr << 1)) << 4) + (p4 & 1) * 8;
    Anch au[4];
#pragma unroll
    for (int ks = 0; ks < 4; ++ks) au[ks] = mk_anch(sm0 + tk * 256 + (((4 * ks + g) ^ tk) << 4));
    const Anch aw = mk_anch(sm0 + wa);
    f32x4 acc[5] = {};
    struct Frag { bf16x8 w, u[5]; } F0, F1;
    auto step = [&](const Frag& C, Frag& N, int t, int ks, bool more) {
        const int sv = E_W + (t % 5) * 16384 + ks * 4096, su = (t & 3) * 20480;
        if (more) N.w = ldtr2(at(aw, sv), at(aw, sv + 512));
        SBAR();
#pragma unroll
        for (int j = 0; j < 5; ++j) {
            acc[j] = mfma16(C.u[j], C.w, acc[j]); SBAR();
            if (more) N.u[j] = ld128(at(au[ks], su + j * 4096));
            SBAR();
        }
    };
    lds_bar();
    u32x2 hv[5];
    unsigned b2u;
    {
        const rsrc_t rh = mk_rsrc(p.ws + WS_H, SEQ * DM * 2), rb2 = mk_rsrc(p.b2, SEQ * 2);
#pragma unroll
        for (int j = 0; j < 5; ++j) hv[j] = ld64g<16>(rh, (srow * DM + d0 + 16 * j + 4 * g) * 2);
        b2u = __builtin_amdgcn_raw_buffer_load_b16(rb2, srow * 2, 0, 0);
    }
    F0.w = ldtr2(at(aw, E_W), at(aw, E_W + 512));
#pragma unroll
    for (int j = 0; j < 5; ++j) F0.u[j] = ld128(at(au[0], j * 4096));
#pragma unroll
    for (int t = 0; t < E_NT; ++t) {
        step(F0, F1, t, 1, true);
        step(F1, F0, t, 2, true);
        step(F0, F1, t, 3, true);
        if (t + 1 < E_NT) lds_bar();
        step(F1, F0, t + 1, 0, t + 1 < E_NT);
    }
    {
        const rsrc_t ro = mk_rsrc(p.out, SEQ * DM * 2);
        const float bias = bf_lo(b2u);
        LAS char* stg = sm + w * 2560;
#pragma unroll
        for (int j = 0; j < 5; ++j) {
            const f32x4 a = acc[j];
            u32x2 v;
            v[0] = cvtpk(bf_lo(hv[j][0]) + (a[0] + bias), bf_hi(hv[j][0]) + (a[1] + bias));
            v[1] = cvtpk(bf_lo(hv[j][1]) + (a[2] + bias), bf_hi(hv[j][1]) + (a[3] + bias));
            *(LAS u32x2*)(stg + tk * 160 + j * 32 + g * 8) = v;
        }
        asm volatile("s_waitcnt lgkmcnt(0)" ::: "memory");
#pragma unroll
        for (int j = 0; j < 3; ++j) {
            const int ch = j * 64 + l, rr = ch / 10, c = ch - rr * 10;
            if (j < 2 || l < 32) __builtin_amdgcn_raw_buffer_store_b128(*(const LAS u32x4*)(stg + ch * 16), ro, ((s0 + 16 * w + rr) * DM + d0) * 2 + c * 16, 0, OUT_AUX);
        }
    }
}

constexpr int LDS_BYTES = 163840;
extern "C" __global__ void __launch_bounds__(512, 2) k_block(Params p) {
    extern __shared__ __attribute__((aligned(16))) char smem[];
    LAS char* sm = (LAS char*)smem;
    const int b = blockIdx.x;
    unsigned code_touch = 0;
    if (uni(threadIdx.x >> 6) == 3) {
        unsigned long long pc; unsigned here, len;
        asm volatile("s_getpc_b64 %0\n.Lcp%=:\n\ts_mov_b32 %1, .Lcp%=-k_block\n\ts_mov_b32 %2, k_block_last-k_block" : "=s"(pc), "=s"(here), "=s"(len));
        const unsigned per = (len / 128 + 31) / 32, line = (b >> 3) * per + (threadIdx.x & 63);
        code_touch = __builtin_amdgcn_raw_buffer_load_b32(mk_rsrc((const void*)(pc - here), len), (threadIdx.x & 63) < per ? line * 128 : 0xffffff00u, 0, 0);
    }
    unsigned sync_touch = 0;
    if (uni(threadIdx.x >> 6) == 2) {
        constexpr unsigned per = ((WS_END - WS_ST) / 128 + 255) / 256;
        sync_touch = __builtin_amdgcn_raw_buffer_load_b32(mk_rsrc(p.ws + WS_ST, WS_END - WS_ST), (threadIdx.x & 63) < per ? (b * per + (threadIdx.x & 63)) * 128 : 0xffffff00u, 0, 0);
    }
    const unsigned ep = __hip_atomic_load((GAS unsigned*)(p.ws + WS_EP), __ATOMIC_RELAXED, __HIP_MEMORY_SCOPE_AGENT);
    unsigned tag;
    phaseA(p, sm, b, ep, tag);
    asm volatile("" ::"v"(code_touch));
    asm volatile("" ::"v"(sync_touch));
    phaseB(p, sm, b, tag);
    phaseC(p, sm, b, tag);
    phaseD(p, sm, b, tag);
    phaseE(p, sm, b, tag);
    asm volatile("k_block_last:");
    if (b == 0 && threadIdx.x == 0) __hip_atomic_store((GAS unsigned*)(p.ws + WS_EP), tag, __ATOMIC_RELAXED, __HIP_MEMORY_SCOPE_AGENT);
}

extern "C" void kernel_launch(void* const* d_in, const int* in_sizes, int n_in, void* d_out, int out_size, void* d_ws, size_t ws_size,
                              hipStream_t stream) {
    Params p;
    p.x = (const uint16_t*)d_in[0]; p.wqkv = (const uint16_t*)d_in[1]; p.wo = (const uint16_t*)d_in[2];
    p.w1 = (const uint16_t*)d_in[4]; p.b1 = (const uint16_t*)d_in[5]; p.w2 = (const uint16_t*)d_in[6]; p.b2 = (const uint16_t*)d_in[7];
    p.out = (uint16_t*)d_out; p.ws = (char*)d_ws;
    (void)hipFuncSetAttribute((const void*)k_block, hipFuncAttributeMaxDynamicSharedMemorySize, LDS_BYTES);
    k_block<<<256, 512, LDS_BYTES, stream>>>(p);
}
```
